```python
import torch

def _softmax(x, axis=-1):
    return torch.softmax(x, dim=axis)
def _gelu(x):
    return torch.nn.functional.gelu(x, approximate="tanh")
def _silu(x):
    return torch.nn.functional.silu(x)
def _sigmoid(x):
    return torch.sigmoid(x)
def _layernorm(x, axis=-1, eps=1e-5):
    m = x.mean(dim=axis, keepdim=True)
    v = x.var(dim=axis, keepdim=True, unbiased=False)
    return (x - m) / torch.sqrt(v + eps)
def _scatter_add(tgt, idx, src, axis=0):
    return tgt.clone().index_add_(axis, idx, src)
def _segment_sum(src, seg, n):
    out = torch.zeros((n,) + src.shape[1:], dtype=src.dtype)
    return out.index_add_(0, seg, src)
def _pad(x, pw):
    flat = tuple(v for pair in reversed(pw) for v in pair)
    return torch.nn.functional.pad(x, flat)

kernel_name = 'interleave_layers__ssm_conv_h3__glu_gated_reglu__prompt'

def setup_inputs():

    _w = (lambda a: (lambda t: t.to(torch.bfloat16) if t.is_floating_point() else t)(torch.as_tensor(a)))
    def _A_setup():
        B, S, D = 1, 2048, 448
        ks = 4
        return {
            "q": _w((lambda *s: torch.randn(*s, dtype=torch.bfloat16) * 0.05)(B, S, D)),
            "k": _w((lambda *s: torch.randn(*s, dtype=torch.bfloat16) * 0.2)(B, S, D)),
            "v": _w((lambda *s: torch.randn(*s, dtype=torch.bfloat16) * 0.2)(B, S, D)),
            "shift_k": _w((lambda *s: torch.randn(*s, dtype=torch.bfloat16) * 0.2)(D, ks) * 0.5),
            "ssm_k": _w((lambda *s: torch.randn(*s, dtype=torch.bfloat16) * 0.2)(D, S) * (1.0 / S ** 0.5)),
            "ssm_D": _w((lambda *s: torch.randn(*s, dtype=torch.bfloat16) * 0.2)(D)),
        }
    _A_in = _A_setup()

    def _B_setup():
        B, S, D, F = 1, 2048, 448, 1792
        return {
            "x": _w((lambda *s: torch.randn(*s, dtype=torch.bfloat16) * 0.05)(B, S, D)),
            "W_gate": _w((lambda *s: torch.randn(*s, dtype=torch.bfloat16) * 0.2)(D, F) * (2.0 / D) ** 0.5),
            "W_up": _w((lambda *s: torch.randn(*s, dtype=torch.bfloat16) * 0.2)(D, F) * (2.0 / D) ** 0.5),
            "W_down": _w((lambda *s: torch.randn(*s, dtype=torch.bfloat16) * 0.2)(F, D) * (1.0 / F) ** 0.5),
        }
    _B_in = _B_setup()

    return {
        "A_q": _A_in["q"],
        "A_k": _A_in["k"],
        "A_v": _A_in["v"],
        "A_shift_k": _A_in["shift_k"],
        "A_ssm_k": _A_in["ssm_k"],
        "A_ssm_D": _A_in["ssm_D"],
        "B_x": _B_in["x"],
        "B_W_gate": _B_in["W_gate"],
        "B_W_up": _B_in["W_up"],
        "B_W_down": _B_in["W_down"],
    }

def reference(A_q, A_k, A_v, A_shift_k, A_ssm_k, A_ssm_D, B_x, B_W_gate, B_W_up, B_W_down):
    def _A_ref(q, k, v, shift_k, ssm_k, ssm_D):
        B, S, D = q.shape
        ks = shift_k.shape[-1]
        k_pad = _pad(k, ((0, 0), (ks - 1, 0), (0, 0)))
        k_shift = torch.zeros_like(k)
        for t in range(ks):
            k_shift = k_shift + k_pad[:, t:t + S, :] * shift_k[:, ks - 1 - t][None, None, :]
        kv = k_shift * v
        L = 2 * S
        kv_pad = _pad(kv, ((0, 0), (0, S), (0, 0)))
        kf_pad = _pad(ssm_k, ((0, 0), (0, S)))
        kv_f = (lambda x, axes=(-2,-1): torch.fft.rfft2(x.float(), dim=axes))(kv_pad[:, None, :, :], axes=(1, 2))
        kf_f = (lambda x, axes=(-2,-1): torch.fft.rfft2(x.float(), dim=axes))(kf_pad[:, None, :], axes=(1, 2))
        kf_f = (lambda x, *d: x.permute(*d))(kf_f, 1, 2, 0)
        y_f = kv_f * kf_f[None, :, :, :]
        y = (lambda x, s, axes=(-2,-1): torch.fft.irfft2(x, s=s, dim=axes).to(torch.bfloat16))(y_f, (1, L), axes=(1, 2))[:, 0, :S, :]
        y = y + kv * ssm_D[None, None, :]
        return q * y

    def _B_ref(x, W_gate, W_up, W_down):
        gate = torch.einsum("bsd,df->bsf", x, W_gate)
        up = torch.einsum("bsd,df->bsf", x, W_up)
        gate = gate * (lambda x, dt: x.to(dt))(gate > 0, gate.dtype)
        h = gate * up
        return torch.einsum("bsf,fd->bsd", h, W_down)

    x = A_q
    x = x + _A_ref(_layernorm(x, axis=-1), A_k, A_v, A_shift_k, A_ssm_k, A_ssm_D)
    x = x + _B_ref(_layernorm(x, axis=-1), B_W_gate, B_W_up, B_W_down)
    x = x + _A_ref(_layernorm(x, axis=-1), A_k, A_v, A_shift_k, A_ssm_k, A_ssm_D)
    x = x + _B_ref(_layernorm(x, axis=-1), B_W_gate, B_W_up, B_W_down)
    return x
```

```cpp
#define FRONT_THR 40
#include <hip/hip_runtime.h>
#include <hip/hip_bf16.h>
#include <stdint.h>
#pragma clang diagnostic ignored "-Wint-to-pointer-cast"

#define GAS __attribute__((address_space(1)))
#define LAS __attribute__((address_space(3)))
typedef unsigned short u16;
typedef unsigned v4u __attribute__((ext_vector_type(4)));
typedef unsigned v2u __attribute__((ext_vector_type(2)));
typedef float f32x4 __attribute__((ext_vector_type(4)));
typedef float f32x2 __attribute__((ext_vector_type(2)));
typedef float f32x16 __attribute__((ext_vector_type(16)));
typedef __bf16 bf16x8 __attribute__((ext_vector_type(8)));
typedef __bf16 bf16x2 __attribute__((ext_vector_type(2)));
typedef short s16x4 __attribute__((ext_vector_type(4)));

constexpr int S = 2048, D = 448, F = 1792;
constexpr int NFT = F / 16;
constexpr int KS1 = D / 16;
constexpr int NCT = D / 16;
constexpr int KS2 = F / 32;

constexpr unsigned OFF_G = 0;
constexpr unsigned OFF_CF = (3u << 20) + 131072;
constexpr unsigned OFF_WF = 36864;
constexpr unsigned OFF_AF = 3u << 20;
constexpr unsigned OFF_XF = 65536;
constexpr unsigned OFF_HF = 1u << 20;
constexpr unsigned OFF_GR = 37u << 20;
constexpr unsigned GR_KIND = 128 * 28 * 128;
constexpr unsigned OFF_UREV = 4u << 20;
constexpr unsigned OFF_X0T = 6u << 20;
constexpr unsigned OFF_YB = 8u << 20;
constexpr unsigned OFF_X1B = 10u << 20;
constexpr unsigned OFF_X3 = 12u << 20;
constexpr unsigned OFF_STAT0 = 14u << 20;
constexpr unsigned OFF_CS = OFF_STAT0 + 65536;
constexpr unsigned OFF_WGU = 15u << 20;
constexpr unsigned OFF_WD = 19u << 20;
constexpr unsigned OFF_H = 21u << 20;
constexpr unsigned H_LAYER = 8u << 20;
constexpr unsigned WS_USED = 40u << 20;

__device__ __forceinline__ float u2f(unsigned u) { return __builtin_bit_cast(float, u); }
__device__ __forceinline__ unsigned f2u(float f) { return __builtin_bit_cast(unsigned, f); }
__device__ __forceinline__ float bf2f(unsigned u) { return __builtin_bit_cast(float, u << 16); }
__device__ __forceinline__ float bflo(unsigned u) { return __builtin_bit_cast(float, u << 16); }
__device__ __forceinline__ float bfhi(unsigned u) { return __builtin_bit_cast(float, u & 0xFFFF0000u); }
__device__ __forceinline__ unsigned pack2(float lo, float hi) {
  bf16x2 v = {(__bf16)lo, (__bf16)hi};
  return __builtin_bit_cast(unsigned, v);
}
__device__ __forceinline__ float rb(float x) { return bf2f((unsigned)__builtin_bit_cast(u16, (__bf16)x)); }
__device__ __forceinline__ f32x16 mfma32(v4u a, v4u b, f32x16 c) {
  return __builtin_amdgcn_mfma_f32_32x32x16_bf16(__builtin_bit_cast(bf16x8, a), __builtin_bit_cast(bf16x8, b), c, 0, 0, 0);
}
__device__ __forceinline__ f32x4 mfma16(v4u a, v4u b, f32x4 c) {
  return __builtin_amdgcn_mfma_f32_16x16x32_bf16(__builtin_bit_cast(bf16x8, a), __builtin_bit_cast(bf16x8, b), c, 0, 0, 0);
}
__device__ __forceinline__ float dot2(unsigned a, unsigned b, float c) {
  return __builtin_amdgcn_fdot2_f32_bf16(__builtin_bit_cast(bf16x2, a), __builtin_bit_cast(bf16x2, b), c, false);
}
__device__ __forceinline__ float ln_rstd(float var) {
  const float v = rb(rb(var) + 1e-5f);
  return __builtin_amdgcn_rcpf(rb(__builtin_amdgcn_sqrtf(v)));
}
__device__ __forceinline__ float fold32(float a, float b) {
  const auto r = __builtin_amdgcn_permlane32_swap(f2u(a), f2u(b), false, false);
  return u2f(r[0]) + u2f(r[1]);
}
__device__ __forceinline__ float fold16(float a) {
  const auto r = __builtin_amdgcn_permlane16_swap(f2u(a), f2u(a), false, false);
  return u2f(r[0]) + u2f(r[1]);
}

template <int CTRL> __device__ __forceinline__ float dppf(float v) { return u2f((unsigned)__builtin_amdgcn_update_dpp(0, (int)f2u(v), CTRL, 0xF, 0xF, true)); }
struct P {
  const u16 *q, *k, *v, *sk, *hk, *sD, *Wg, *Wu, *Wd;
  u16* out;
  unsigned char* ws;
};
constexpr unsigned L_XS = 0, XS_SLOT = 28672;
constexpr unsigned L_WD = 57344;
constexpr unsigned L_RED = 143360;
constexpr unsigned L_ST = L_RED + 16384;
constexpr unsigned L_TOTAL = L_ST + 1024;
constexpr int CV_HS = 4608, CV_UC = 4160, CV_CH = CV_HS + 4 * CV_UC;

#define OUTMODE 1
#define OUTPOL " sc1"
#define SPINSL "2"
#define WAITV(n) __builtin_amdgcn_s_waitcnt(((n) & 15) | (((n) >> 4) << 14) | (7 << 4) | (15 << 8))
#define WAITL(n) __builtin_amdgcn_s_waitcnt(15 | (3 << 14) | (7 << 4) | ((n) << 8))
#define SBAR() asm volatile("s_barrier")
#define BARRIER() asm volatile("s_waitcnt lgkmcnt(0)\n\ts_barrier" ::: "memory")
#define SB() __builtin_amdgcn_sched_barrier(0)
#define NOPS() asm volatile("s_nop 7\n\ts_nop 7")
#define BLOAD4(dst, voff, soff, imm) asm volatile("buffer_load_dwordx4 %0, %1, %2, %3 offen offset:" #imm " sc1" : "=v"(dst) : "v"(voff), "s"(srd), "s"(soff))
#define BLOADWD(dst, lit) do { unsigned so_; asm volatile("s_add_u32 %1, %4, " #lit "\n\tbuffer_load_dwordx4 %0, %2, %3, %1 offen sc1" : "=a"(dst), "=&s"(so_) : "v"(v_l16), "s"(srd), "s"(sWd) : "scc"); } while (0)
#define BLOAD2(dst, voff, soff) asm volatile("buffer_load_dwordx2 %0, %1, %2, %3 offen sc1" : "=v"(dst) : "v"(voff), "s"(srd), "s"(soff))
#define BSTORE4(src, voff, soff) asm volatile("buffer_store_dwordx4 %0, %1, %2, %3 offen sc1\n\ts_nop 1" :: "v"(src), "v"(voff), "s"(srd), "s"(soff))
#define BSTORE2(src, voff, soff) asm volatile("buffer_store_dwordx2 %0, %1, %2, %3 offen sc1" :: "v"(src), "v"(voff), "s"(srd), "s"(soff))
#define OSTORE2(src, voff, soff) asm volatile("buffer_store_dwordx2 %0, %1, %2, %3 offen" OUTPOL :: "v"(src), "v"(voff), "s"(srdo), "s"(soff))
#define TIE(x) asm volatile("" : "+v"(x))
#define TIE2(x, y) asm volatile("" : "+v"(x), "+v"(y))
#define TIE3(x, y, z) asm volatile("" : "+v"(x), "+v"(y), "+v"(z))
#define TIE4(x, y, z, u) asm volatile("" : "+v"(x), "+v"(y), "+v"(z), "+v"(u))
#define GLDS1(lit, sbase, slit) do { unsigned so_; \
  asm volatile("s_add_u32 m0, %3, " #lit "\n\ts_add_u32 %0, %4, " #slit "\n\tbuffer_load_dwordx4 %1, %2, %0 offen sc1 lds" \
               : "=&s"(so_) : "v"(v_l16), "s"(srd), "s"(ldsx), "s"(sbase) : "scc"); } while (0)
#define CHECKF(pv, voff, soff) CHECKT(tag64, pv, voff, soff)
#define CHECKT(tg, pv, voff, soff) do { unsigned cnt_; \
  asm volatile("v_cmp_ne_u64 vcc, %3, %0\n\ts_cbranch_vccnz .Lsl%=\n.Lbk%=:\n\t.subsection 1\n.Lsl%=:\n\ts_movk_i32 %1, 0x4000\n" \
               ".Llp%=:\n\ts_sleep " SPINSL "\n\tbuffer_load_dwordx2 %0, %2, %4, %5 offen sc1\n\ts_waitcnt vmcnt(0)\n\t" \
               "v_cmp_ne_u64 vcc, %3, %0\n\ts_cbranch_vccz .Lbk%=\n\ts_sub_u32 %1, %1, 1\n\ts_cbranch_scc0 .Llp%=\n\ts_branch .Lbk%=\n\t.subsection 0" \
               : "+v"(pv), "=&s"(cnt_) : "v"(voff), "s"(tg), "s"(srd), "s"(soff) : "vcc", "scc"); } while (0)
#define GLOADG(g, R0, R3, soff) asm volatile("buffer_load_dwordx4 %0, %1, %2, %3 offen sc1" : "={v[" #R0 ":" #R3 "]}"(g) : "v"(v_gld), "s"(srd), "s"(soff))
#define GCMP(R1, R3) "v_cmp_ne_u32 vcc, %4, v" #R1 "\n\tv_cmp_ne_u32 %2, %4, v" #R3 "\n\ts_or_b64 vcc, vcc, %2\n\ts_and_b64 vcc, vcc, %7\n\t"
#define CHECKG(g, R0, R1, R3, soff) do { unsigned cnt_; unsigned long long t_; \
  asm volatile(GCMP(R1, R3) "s_cbranch_scc1 .Lsl%=\n.Lbk%=:\n\t.subsection 1\n.Lsl%=:\n\ts_movk_i32 %1, 0x4000\n" \
               ".Llp%=:\n\ts_sleep " SPINSL "\n\tbuffer_load_dwordx4 %0, %3, %5, %6 offen sc1\n\ts_waitcnt vmcnt(0)\n\t" \
               GCMP(R1, R3) "s_cbranch_scc0 .Lbk%=\n\ts_sub_u32 %1, %1, 1\n\ts_cbranch_scc0 .Llp%=\n\ts_branch .Lbk%=\n\t.subsection 0" \
               : "+{v[" #R0 ":" #R3 "]}"(g), "=&s"(cnt_), "=&s"(t_) : "v"(v_gld), "s"(e1), "s"(srd), "s"(soff), "s"(0x3FFF3FFF3FFF3FFFull) : "vcc", "scc"); } while (0)
#define DSR128(dst, addr, off) asm volatile("ds_read_b128 %0, %1 offset:" #off : "=v"(dst) : "v"(addr))
#define DSRTR(dst, addr, off, off2) do { v2u lo_, hi_; \
  asm volatile("ds_read_b64_tr_b16 %0, %2 offset:" #off "\n\tds_read_b64_tr_b16 %1, %2 offset:" #off2 : "=&v"(lo_), "=&v"(hi_) : "v"(addr)); \
  dst = __builtin_shufflevector(lo_, hi_, 0, 1, 2, 3); } while (0)
#define DSW128(addr, src, off) asm volatile("ds_write_b128 %0, %1 offset:" #off :: "v"(addr), "v"(src))
#define DSW32(addr, src, off) asm volatile("ds_write_b32 %0, %1 offset:" #off :: "v"(addr), "v"(src))
#define MF32(con, acc, a, b) asm volatile("v_mfma_f32_32x32x16_bf16 %0, %1, %2, %0" : "+v"(acc) : con(a), "v"(b))
#define MF32Z(con, acc, a, b) asm volatile("v_mfma_f32_32x32x16_bf16 %0, %1, %2, 0" : "=&v"(acc) : con(a), "v"(b))
#define MF16(con, acc, a, b) asm volatile("v_mfma_f32_16x16x32_bf16 %0, %1, %2, %0" : "+v"(acc) : con(a), "v"(b))
#define MF16Z(con, acc, a, b) asm volatile("v_mfma_f32_16x16x32_bf16 %0, %1, %2, 0" : "=&v"(acc) : con(a), "v"(b))
#define DSRU16(dst, addr, off) asm volatile("ds_read_u16 %0, %1 offset:" #off : "=v"(dst) : "v"(addr))
#define GLDSR(lit, slit) do { unsigned so_; \
  asm volatile("s_add_u32 m0, %3, " #lit "\n\ts_add_u32 %0, %4, " #slit "\n\tbuffer_load_dwordx4 %1, %2, %0 offen sc1 lds" \
               : "=&s"(so_) : "v"(v_res), "s"(srd), "s"(ldsr), "s"(sRes) : "scc"); } while (0)
#define GLDSW(lit, sbase, slit) do { unsigned so_; \
  asm volatile("s_add_u32 m0, %3, " #lit "\n\ts_add_u32 %0, %4, " #slit "\n\t" \
               "buffer_load_dwordx4 %1, %2, %0 offen sc1 lds" \
               : "=&s"(so_) : "v"(v_l16), "s"(srd), "s"(ldsw), "s"(sbase) : "scc"); } while (0)

#define FRONT_PACE0 1
#define FRONT_PACE1 1
#define SCOUT_D0 12
#define SCOUT_D1 40
#define TOUCHWS 0
#define GRP 8
#define PFSTR 128
#define FRONT_GAPH 0
#define FRONT_GAP 0
#define FRONT_N1 56
constexpr unsigned F_STG = 0;
constexpr unsigned F_HS = 114688;
constexpr unsigned U0S = 4288;
constexpr unsigned F_U0 = F_HS + 2 * 4608;
constexpr unsigned F_UC = F_U0 + 2 * U0S + 192;
constexpr unsigned F_TR = F_UC;
static_assert(F_UC + 6 * CV_UC + 32 <= L_TOTAL && F_U0 % 256 == 0 && F_UC % 256 == 64, "front LDS map");
#define MARK(t) asm volatile("; " t)
#define GLDSX(sd, ldsa, voff, soff, imm, pol) asm volatile("s_mov_b32 m0, %2\n\ts_nop 0\n\tbuffer_load_dwordx4 %0, %1, %3 offen offset:" #imm pol " lds" :: "v"(voff), "s"(sd), "s"(ldsa), "s"(soff) : "memory")
#define TIEA(x) asm volatile("" : "+a"(x))
#define STUB(k, nxt) asm volatile(".Lstub" #k ":\n\ts_cbranch_execz " nxt)
#define GM() asm volatile("")
#define HOP4(k, k1, k2, k3) asm volatile("s_mov_b64 exec, 0\n\ts_cmp_eq_u32 %0, 1\n\ts_cbranch_scc1 .Lstub" #k1 "\n\ts_cmp_eq_u32 %0, 2\n\ts_cbranch_scc1 .Lstub" #k2 "\n\ts_cmp_eq_u32 %0, 3\n\ts_cbranch_scc1 .Lstub" #k3 "\n\ts_branch .Lstub" #k "\n.Lwret" #k ":\n.Lwret" #k1 ":\n.Lwret" #k2 ":\n.Lwret" #k3 ":\n\ts_mov_b64 exec, -1" :: "s"(w) : "scc")
#define HOP(k) asm volatile("s_mov_b64 exec, 0\n\ts_branch .Lstub" #k "\n.Lwret" #k ":\n\ts_mov_b64 exec, -1")
#define DSR64(dst, addr, off) asm volatile("ds_read_b64 %0, %1 offset:" #off : "=v"(dst) : "v"(addr))
#define DSRTRW(con, dst, addr, off, off2) do { v2u lo_, hi_; \
  asm volatile("ds_read_b64_tr_b16 %0, %2 offset:" #off "\n\tds_read_b64_tr_b16 %1, %2 offset:" #off2 : con(lo_), con(hi_) : "v"(addr)); \
  dst = __builtin_shufflevector(lo_, hi_, 0, 1, 2, 3); } while (0)
#define MF16B(con, acc, a, b) asm volatile("v_mfma_f32_16x16x32_bf16 %0, %1, %2, %0" : "+v"(acc) : "v"(a), con(b))
__device__ __forceinline__ v4u mksrd(const void* ptr, unsigned bytes) {
  return v4u{(unsigned)(size_t)ptr, (unsigned)((size_t)ptr >> 32) & 0xFFFFu, bytes, 0x00020000u};
}

__device__ __forceinline__ void conv_epi(f32x4 y, v2u ur, v2u x0, v4u s0, v4u s1, float Dv, v2u& ox, v2u& oy) {
  const float u[4] = {bfhi(ur[1]), bflo(ur[1]), bfhi(ur[0]), bflo(ur[0])};
  const float xf[4] = {bflo(x0[0]), bfhi(x0[0]), bflo(x0[1]), bfhi(x0[1])};
  oy = v2u{pack2(fmaf(u[0], Dv, y[0]), fmaf(u[1], Dv, y[1])), pack2(fmaf(u[2], Dv, y[2]), fmaf(u[3], Dv, y[3]))};
  const float Y[4] = {bflo(oy[0]), bfhi(oy[0]), bflo(oy[1]), bfhi(oy[1])};
  const unsigned ms[4] = {s0[0], s0[2], s1[0], s1[2]};
  float x1[4];
#pragma unroll
  for (int j = 0; j < 4; ++j) x1[j] = fmaf((xf[j] - bflo(ms[j])) * __builtin_amdgcn_rcpf(bfhi(ms[j])), Y[j], xf[j]);
  ox = v2u{pack2(x1[0], x1[1]), pack2(x1[2], x1[3])};
}
__device__ __forceinline__ v4u widen(v2u lo, v2u hi) {
  const auto r0 = __builtin_amdgcn_permlane16_swap(lo[0], hi[0], false, false), r1 = __builtin_amdgcn_permlane16_swap(lo[1], hi[1], false, false);
  return v4u{r0[0], r1[0], r0[1], r1[1]};
}
#define MFC(z, acc, lo, hi, con, bfr) do { const v4u a_ = __builtin_shufflevector(lo, hi, 0, 1, 2, 3); MFC##z(acc, a_, con, bfr); } while (0)
#define MFC0(acc, a, con, b) asm volatile("v_mfma_f32_16x16x32_bf16 %0, %1, %2, %0" : "+v"(acc) : "v"(a), con(b))
#define MFC1(acc, a, con, b) asm volatile("v_mfma_f32_16x16x32_bf16 %0, %1, %2, 0" : "=&v"(acc) : "v"(a), con(b))
__device__ __forceinline__ void spare_role(const P& p, unsigned char* smem, unsigned lb, int s, int tid, int lane, int w, v4u srd, v2u tagv, unsigned v_one, unsigned long long tag64) {
  MARK("ROLE spare");
  const unsigned vl = lane * 16;
  WAITV(0);
  BARRIER();
  if (s < 28) {
    const unsigned ra = lb + (8 * (lane >> 4) + ((lane & 15) >> 2)) * 896 + 8 * (lane & 3);
#pragma unroll
    for (int i = 0; i < 14; ++i) {
      const int fi = 14 * w + i, ks = fi / 28, ctile = fi % 28;
      const unsigned a = ra + ks * (32 * 896) + ctile * 32;
      const v2u r0 = __builtin_bit_cast(v2u, __builtin_amdgcn_ds_read_tr16_b64_v4i16((LAS s16x4*)a));
      const v2u r1 = __builtin_bit_cast(v2u, __builtin_amdgcn_ds_read_tr16_b64_v4i16((LAS s16x4*)(a + 4 * 896)));
      const v4u o = {r0[0], r0[1], r1[0], r1[1]};
      const unsigned so = OFF_WD + (ctile * KS2 + 2 * s + ks) * 1024;
      BSTORE4(o, vl, so);
    }
    WAITV(0);
    { const unsigned so = OFF_WF + (s * 4 + w) * 128; BSTORE2(tagv, v_one, so); }
  }
}

__global__ void __launch_bounds__(256, 1) __attribute__((aligned(4096))) k_main(P p) {
  extern __shared__ __attribute__((aligned(16))) unsigned char smem[];
  const unsigned lb = (unsigned)(size_t)(LAS unsigned char*)smem;
  const int tid = threadIdx.x, lane = tid & 63, w = __builtin_amdgcn_readfirstlane(tid >> 6);
  const int b = (int)blockIdx.x - 32, team = b & 7, mem = b >> 3, q = mem / 7, ds = mem % 7;
  const int n1 = 28 * (team & (GRP - 1)) + mem, team1 = (team & ~(GRP - 1)) + (n1 & (GRP - 1)), mem1 = n1 / GRP;
  unsigned dummy = 0;
  {
    unsigned long long pc; unsigned len;
    asm volatile("s_getpc_b64 %0\n.Lcode_top:\n\ts_mov_b32 %1, .Lcode_end-.Lcode_top" : "=s"(pc), "=s"(len));
    const v4u sc = {(unsigned)pc, (unsigned)(pc >> 32) & 0xFFFFu, len, 0x00020000u};
    const unsigned vo = lane * PFSTR, so = ((blockIdx.x >> 3) * 4 + w) * (64 * PFSTR);
    asm volatile("buffer_load_dword %0, %1, %2, %3 offen" : "+v"(dummy) : "v"(vo), "s"(sc), "s"(so));
  }
  unsigned G, dpair;
  {
    const u16* dp_ = p.sD + 2 * (b < 0 ? 0 : b);
    asm volatile("s_load_dword %0, %2, 0x0\n\ts_load_dword %1, %3, 0x0" : "=&s"(G), "=&s"(dpair) : "s"(p.ws), "s"(dp_));
  }
  const v4u srd = mksrd(p.ws, WS_USED);
  const unsigned v_l16 = lane * 16;
  v4u xr[4];
  const v4u rqq = mksrd(p.q, S * D * 2);
#define PL(dst, vo, sd, so, imm) asm volatile("buffer_load_dwordx4 %0, %1, %2, %3 offen offset:" #imm : "=v"(dst) : "v"(vo), "s"(sd), "s"(so))
#define QROWS() do { const unsigned vq = (unsigned)(lane >> 4) * (D * 2) + (lane & 15) * 16, vq3 = (lane & 8) ? 0x80000000u : vq, sq_ = (blockIdx.x * 8 + (w & 1) * 4) * (D * 2); \
    PL(xr[0], vq, rqq, sq_, 0); PL(xr[1], vq, rqq, sq_, 256); PL(xr[2], vq, rqq, sq_, 512); PL(xr[3], vq3, rqq, sq_, 768); } while (0)
  const bool actA = b >= 0 && w < 2;
  const int rt = (int)((unsigned)b * 293u >> 11), cg = 2 * (b - 7 * rt) + w;
  const int chunk = lane & 3, rq = lane >> 2;
  v4u kk[7], vv[4], qq[4], sk[4];
  if (actA) {
      MARK("ROLE w01");
      {
        const unsigned v1 = (unsigned)(rq * 4 * D + 8 * chunk) * 2, sb = (unsigned)(64 * rt * D + 32 * cg) * 2;
        const unsigned v0 = (rt | rq) ? v1 + sb - 3 * D * 2 : 0x80000000u, vs_ = (unsigned)chunk * 64, ss_ = cg * 256, z_ = 0;
        const v4u rk = mksrd(p.k, S * D * 2), rv = mksrd(p.v, S * D * 2), rs = mksrd(p.sk, D * 8);
        PL(kk[3], v1, rk, sb, 0); PL(kk[4], v1, rk, sb, 896); PL(kk[5], v1, rk, sb, 1792); PL(kk[6], v1, rk, sb, 2688);
        PL(vv[0], v1, rv, sb, 0); PL(vv[1], v1, rv, sb, 896); PL(vv[2], v1, rv, sb, 1792); PL(vv[3], v1, rv, sb, 2688);
        PL(kk[0], v0, rk, z_, 0); PL(kk[1], v0, rk, z_, 896); PL(kk[2], v0, rk, z_, 1792);
        PL(sk[0], vs_, rs, ss_, 0); PL(sk[1], vs_, rs, ss_, 16); PL(sk[2], vs_, rs, ss_, 32); PL(sk[3], vs_, rs, ss_, 48);
        PL(qq[0], v1, rqq, sb, 0); PL(qq[1], v1, rqq, sb, 896); PL(qq[2], v1, rqq, sb, 1792); PL(qq[3], v1, rqq, sb, 2688);
        QROWS();
      }
      SB();
      {
        const v4u sh = mksrd(p.hk, D * S * 2);
        const unsigned la = __builtin_amdgcn_readfirstlane(lb + F_HS + w * 4608 + 480), so = (2 * b + w) * (S * 2);
        GLDSX(sh, la, v_l16, so, 0, ""); GLDSX(sh, la, v_l16, so, 1024, ""); GLDSX(sh, la, v_l16, so, 2048, ""); GLDSX(sh, la, v_l16, so, 3072, "");
      }
  }
  if (b >= 0 && w >= 2) {
    MARK("ROLE w23");
    const int mat = w & 1, r8 = lane >> 3, cp = lane & 7;
    const v4u sw = mksrd(mat ? p.Wu : p.Wg, D * F * 2);
    const unsigned wvo = (unsigned)r8 * (F * 2) + (((cp >> 1) ^ (((r8 >> 1) & 1) | (mat << 1))) * 2 + (cp & 1)) * 16;
    const unsigned wla = lb + F_STG + mat * 57344, wso = mem1 * 128;
    unsigned pc_ = (n1 & (GRP - 1)) * (8 / GRP);
    __builtin_amdgcn_s_sleep(FRONT_PACE0);
#pragma unroll 1
    for (int i = 0; i < 8; ++i) {
      MARK("TRIP 8");
      unsigned la = wla + pc_ * 7168, so = wso + pc_ * (56 * F * 2);
#pragma unroll
      for (int j = 0; j < 7; ++j) { GLDSX(sw, la, wvo, so, 0, ""); la += 1024; so += 8 * F * 2; }
      pc_ = (pc_ + 1) & 7;
      if (i == 0) __builtin_amdgcn_s_sleep(FRONT_GAP);
      WAITV(FRONT_THR);
    }
  }
  if (b < 0) {
    MARK("ROLE spare");
    QROWS();
    const v4u sw = mksrd(p.Wd, F * D * 2);
    unsigned la = lb + w * 14336, so = blockIdx.x * 57344 + w * 14336;
#pragma unroll 1
    for (int i = 0; i < 14; ++i) { MARK("TRIP 14"); GLDSX(sw, la, v_l16, so, 0, ""); la += 1024; so += 1024; }
    WAITV(14);
  }
  if (actA) {
      MARK("ROLE w01");
      if (lane < 30) *(v4u*)(smem + F_HS + w * 4608 + 16 * lane) = v4u{0, 0, 0, 0};
      if (lane < 8) *(v4u*)(smem + F_U0 + w * U0S + 4096 + 16 * lane) = v4u{0, 0, 0, 0};
      WAITV(8); SB();
      TIE4(kk[0], kk[1], kk[2], kk[3]); TIE3(kk[4], kk[5], kk[6]); TIE4(vv[0], vv[1], vv[2], vv[3]); TIE4(sk[0], sk[1], sk[2], sk[3]); TIE4(qq[0], qq[1], qq[2], qq[3]);
      v2u* tb = (v2u*)(smem + F_TR + w * 3 * CV_UC);
#pragma unroll
      for (int j = 0; j < 8; ++j) {
        const unsigned w01 = sk[j >> 1][(j & 1) * 2], w23 = sk[j >> 1][(j & 1) * 2 + 1];
        const float w0 = bflo(w01), w1 = bfhi(w01), w2 = bflo(w23), w3 = bfhi(w23);
        float kf[7], u[4];
#pragma unroll
        for (int i = 0; i < 7; ++i) kf[i] = (j & 1) ? bfhi(kk[i][j >> 1]) : bflo(kk[i][j >> 1]);
#pragma unroll
        for (int r = 0; r < 4; ++r) {
          const float a = fmaf(kf[r + 3], w0, fmaf(kf[r + 2], w1, fmaf(kf[r + 1], w2, kf[r] * w3)));
          u[r] = a * ((j & 1) ? bfhi(vv[r][j >> 1]) : bflo(vv[r][j >> 1]));
        }
        const int row = 8 * chunk + j;
        tb[row * 16 + ((15 - rq) ^ (4 * chunk))] = v2u{pack2(u[3], u[2]), pack2(u[1], u[0])};
        const unsigned sl = (j & 1) ? 0x07060302u : 0x05040100u;
        tb[512 + row * 16 + (rq ^ (4 * chunk))] = v2u{__builtin_amdgcn_perm(qq[1][j >> 1], qq[0][j >> 1], sl), __builtin_amdgcn_perm(qq[3][j >> 1], qq[2][j >> 1], sl)};
      }
      const unsigned vo = (unsigned)(lane >> 3) * (S * 2) + (lane & 7) * 16;
#pragma unroll
      for (int i = 0; i < 4; ++i) {
        const int row = (lane >> 3) + 8 * i, g = lane & 7;
        const v4u a = *(const v4u*)&tb[row * 16 + 2 * (g ^ (2 * i))];
        const v4u x = *(const v4u*)&tb[512 + row * 16 + 2 * (g ^ (2 * i))];
        const unsigned s1 = OFF_UREV + ((32 * cg + 8 * i) * S + (S - 64 - 64 * rt)) * 2, s2 = OFF_X0T + ((32 * cg + 8 * i) * S + 64 * rt) * 2;
        BSTORE4(a, vo, s1);
        BSTORE4(x, vo, s2);
      }
      WAITV(12);
  }
  asm volatile("s_waitcnt lgkmcnt(0)" : "+s"(G), "+s"(dpair));
  const unsigned e1 = G * 0x9E3779B1u + 0x7F4A7C15u, e2 = e1 ^ 0x5BD1E995u;
  const unsigned long long tag64 = ((unsigned long long)e2 << 32) | e1;
  const unsigned long long tag11 = ((unsigned long long)e1 << 32) | e1;
  const unsigned v_one = lane < 16 ? lane * 8u : 0x80000000u;
  const v2u tagv = {e1, e2};
  const unsigned e1v = e1;
  auto xstats = [&]() __attribute__((always_inline)) {
  if (w < 2) {
    MARK("NOT w23");
    TIE4(xr[0], xr[1], xr[2], xr[3]);
    float sum = 0.f, sq = 0.f;
#pragma unroll
    for (int i = 0; i < 4; ++i)
#pragma unroll
      for (int j = 0; j < 4; ++j) sum = dot2(xr[i][j], 0x3F803F80u, sum);
    sum += dppf<0x128>(sum); sum += dppf<0x124>(sum); sum += dppf<0x122>(sum); sum += dppf<0x121>(sum);
    const float mean = sum * (1.0f / D), mean3 = (lane & 8) ? 0.f : mean;
#pragma unroll
    for (int i = 0; i < 4; ++i)
#pragma unroll
      for (int j = 0; j < 4; ++j) { const float mm = i == 3 ? mean3 : mean, a = bflo(xr[i][j]) - mm, c = bfhi(xr[i][j]) - mm; sq = fmaf(a, a, sq); sq = fmaf(c, c, sq); }
    sq += dppf<0x128>(sq); sq += dppf<0x124>(sq); sq += dppf<0x122>(sq); sq += dppf<0x121>(sq);
    const float sd = __builtin_amdgcn_sqrtf(rb(rb(sq * (1.0f / D)) + 1e-5f));
    const v2u o = {pack2(mean, sd), e1};
    const unsigned vo = ((lane & 15) == 0 && tid < 128) ? (unsigned)(blockIdx.x * 8 + (tid >> 4)) * 8 : 0x80000000u, so = OFF_STAT0;
    BSTORE2(o, vo, so);
  }
  };
  if (__builtin_expect(b < 0, 0)) { MARK("ROLE spare"); xstats(); spare_role(p, smem, lb, b + 32, tid, lane, w, srd, tagv, v_one, tag64); asm volatile("" :: "v"(dummy)); return; }
  const int ft = 4 * mem1 + w;
  v4u wg[KS1], wd[14][3];
  const unsigned sWd = OFF_WD + (4 * ds * 56 + 14 * w) * 1024;
  const unsigned selw = (((lane >> 4) & 1) == ((lane >> 2) & 1)) ? 0x3F803F80u : 0u;
  v4u sel = {selw, selw, selw, selw};
  f32x4 csacc = {0.f, 0.f, 0.f, 0.f};
  v4u aext;
  TIE(sel); TIE(csacc);
  const int half = q & 1, qi = q >> 1;
  const v4u srdo = mksrd(p.out, S * D * 2);
  unsigned xl0, xl1, st_wr, st_rd, v_hst, v_pc, v_ph, v_px, v_res, res_rd, v_gst, v_gld, v_xst, v_out, red_rd, red_wr, wd_rd, wa_l;
#define SETUP_MAIN() do { int ln = lane; TIE(ln); const int tok32 = ln & 31, hh = ln >> 5, tok16 = ln & 15, g4 = ln >> 4; \
    xl0 = lb + L_XS + 64 * (8 * hh + ((ln & 15) >> 2)) + 32 * ((ln >> 4) & 1) + 8 * (ln & 3); xl1 = lb + L_XS + ln * 16; \
    st_wr = lb + L_ST + tok32 * 16 + w * 4; st_rd = lb + L_ST + tok32 * 16; \
    v_hst = (unsigned)(tok32 >> 4) * 57344u + ((tok32 & 15) + 16 * (2 * (ft & 1) + hh)) * 16 + (ft >> 1) * 1024; \
    v_pc = (unsigned)(ln < 56 ? ln : 55) * 128; v_ph = (unsigned)(ln < 28 ? ln : 27) * 128; \
    v_px = (unsigned)((ln & 31) < ((w == 0 || w == 3) ? 16 : 24) ? (ln & 31) : 0) * 128 + (ln >> 5) * (8 * 56 * 128); \
    const int tokp = 16 * half + 4 * w + g4, prow = 32 * (w & 1) + (ln >> 1);         \
    v_res = (unsigned)(64 * ds + 4 * (prow & 15) + (prow >> 4)) * 64 + half * 32 + (ln & 1) * 16; \
    res_rd = lb + L_RED + tok16 * 32 + (4 * w + g4) * 2; \
    v_gst = (tok16 & 3) == 0 ? (unsigned)(g4 * 32 + (tok16 >> 2) * 8) : 0x80000000u; \
    v_gld = tok16 < 14 ? (unsigned)((tok16 >> 1) * 128 + g4 * 32 + (tok16 & 1) * 16) : 0x80000000u; \
    v_xst = (unsigned)(tok16 >> 2) * 1024 + (tokp + 32 * ((tok16 >> 1) & 1)) * 16 + 8 * (tok16 & 1); \
    v_out = (unsigned)(tokp * D + 64 * ds + 4 * tok16) * 2; \
    red_rd = lb + L_RED + (4 * w + g4) * 256 + ((tok16 ^ (4 * w + g4)) * 16); \
    red_wr = lb + L_RED + w * 4096 + tok16 * 256 + ((g4 ^ tok16) * 16); \
    wd_rd = lb + L_WD + w * 14336 + ln * 16; \
    const int wmat = (ln >> 4) & 1, wq = (ln & 15) >> 2, wp = ln & 3; \
    wa_l = lb + F_STG + wmat * 57344 + (8 * (ln >> 5) + wq) * 128 + (((w ^ ((wq >> 1) | (wmat << 1))) * 2 + (wp & 1)) * 16) + 8 * (wp >> 1); \
    TIE4(xl0, xl1, st_wr, st_rd); TIE4(v_hst, v_pc, v_ph, v_px); TIE4(v_res, res_rd, v_gst, v_gld); TIE4(v_xst, v_out, red_rd, red_wr); TIE2(wd_rd, wa_l); } while (0)
  const unsigned sCp = OFF_CF + 56 * w * 128, s_pw = OFF_WF + 28 * w * 128, ldsr = lb + L_RED + w * 1024, ldsx = lb + L_XS + w * 7168, ldsw = lb + L_WD + w * 14336;
  v2u pollx, pollh;
  const unsigned sX1 = OFF_X1B + team1 * XS_SLOT + w * 7168, sX3 = OFF_X3 + team1 * XS_SLOT + w * 7168;
  const unsigned sHst = OFF_H + team1 * 114688u, sHf = OFF_HF + (team1 * 112 + ft) * 128;
  const unsigned Tq = 8 * qi + team;
  const unsigned sHp = OFF_HF + (Tq * 112 + 28 * w) * 128, sHl = OFF_H + (2 * Tq + half) * 57344u + 14 * w * 1024;
  const unsigned sXp = OFF_XF + (team1 * 56 + ((7 * w) >> 2) * 8) * 128, sXf = OFF_XF + (Tq * 56 + ds * 8 + half * 4 + w) * 128;
  const unsigned sRes = Tq * XS_SLOT + (w >> 1 ? OFF_YB : OFF_X1B), sGl = OFF_GR + ((2 * Tq + half) * 4 + w) * 896, sGs = sGl + ds * 128;
  const unsigned sX3s = OFF_X3 + (Tq * 28 + 4 * ds) * 1024, sOut = Tq * (32 * D * 2);

  {
    v2u pollf;
    const int ch = w & 1, bh = w >> 1, d = 2 * b + ch;
    const float Dv = ch ? bfhi(dpair) : bflo(dpair);
    unsigned Ab, Bb, Ur, cp_rd, cp_wr, cp_z, vx, vs, v_cv, v_un;
    const unsigned sCf0 = w == 0 ? OFF_CF + b * 128 : 0xF0000000u;
#define SETUP_CONV() do { int t2 = tid; TIE(t2); const int il = t2 & 15, kg = (t2 >> 4) & 3, x00 = 2031 + 8 * kg - il, cpy = x00 & 3; \
      Ab = lb + (cpy ? F_UC + (3 * ch + cpy - 1) * CV_UC : F_U0 + ch * U0S) + 2 * (x00 & ~3) - 64 * 63;     \
      Bb = lb + F_HS + ch * 4608 + 2 * (16 * il + 224 + 8 * kg) - 448;                                      \
      Ur = lb + F_U0 + ch * U0S + 2 * (2044 - 1792 - 16 * il - 4 * kg);                                     \
      const int l2 = t2 & 63; cp_rd = lb + F_U0 + ch * U0S + (t2 >> 7) * 2048 + 16 * l2; cp_wr = lb + F_UC + 3 * ch * CV_UC + (t2 >> 7) * 2048 + 16 * l2; v_un = (bh && l2 == 63) ? 0x80000000u : 16u * l2; \
      cp_z = lb + F_UC + (l2 < 6 ? (3 * ch + (l2 >> 1)) * CV_UC + 4096 + 16 * (l2 & 1) : 6 * CV_UC);                  \
      vx = (16 * il + 4 * kg) * 2; vs = (16 * il + 4 * kg) * 8; \
      v_cv = ((unsigned)((il >> 1) + 8 * (bh ? 1 : 3) * (kg & 1)) * D + d) * 64 + (16 * (il & 1) + 8 * (kg >> 1)) * 2; \
      TIE4(Ab, Bb, Ur, cp_rd); TIE4(cp_wr, cp_z, vx, vs); TIE2(v_cv, v_un); } while (0)
    const unsigned sx = OFF_X0T + d * (S * 2);
    v2u x0[2], ur[2], Al[2][8], Ah[2][8];
    v4u st[2][2], Bk[2][8];
    f32x4 ca0, ca1;
#define SLOADS(cA, cB) do { const unsigned a_ = OFF_STAT0 + (cA) * 2048, b_ = OFF_STAT0 + (cB) * 2048; BLOAD4(st[0][0], vs, a_, 0); BLOAD4(st[0][1], vs, a_, 16); BLOAD4(st[1][0], vs, b_, 0); BLOAD4(st[1][1], vs, b_, 16); } while (0)
#define ELOADS(cA, cB) do { const unsigned a2_ = sx + (cA) * 512, b2_ = sx + (cB) * 512; BLOAD2(x0[0], vx, a2_); BLOAD2(x0[1], vx, b2_); SLOADS(cA, cB); } while (0)
#define BAD2() (__builtin_amdgcn_ballot_w64(((st[0][0][1] ^ e1) | (st[0][0][3] ^ e1) | (st[0][1][1] ^ e1) | (st[0][1][3] ^ e1) | (st[1][0][1] ^ e1) | (st[1][0][3] ^ e1) | (st[1][1][1] ^ e1) | (st[1][1][3] ^ e1)) != 0u) != 0ull)
#define CONV_EPI(cA, cB, vcv, cx, cy, ux, uy) do { \
      TIE4(st[0][0], st[0][1], st[1][0], st[1][1]); \
      if (__builtin_expect(BAD2(), 0)) { MARK("SPIN"); unsigned spin = 0; \
        _Pragma("unroll 1") do { __builtin_amdgcn_s_sleep(4); SLOADS(cA, cB); WAITV(0); TIE4(st[0][0], st[0][1], st[1][0], st[1][1]); } while (BAD2() && ++spin < 0x4000); } \
      TIE4(x0[0], x0[1], ux, uy); TIE2(cx, cy); v2u ox0, oy0, ox1, oy1; \
      conv_epi(cx, ux, x0[0], st[0][0], st[0][1], Dv, ox0, oy0); conv_epi(cy, uy, x0[1], st[1][0], st[1][1], Dv, ox1, oy1); \
      const v4u wx = widen(ox0, ox1), wy = widen(oy0, oy1); const unsigned s1_ = OFF_X1B + (cA) * 8 * XS_SLOT, s2_ = OFF_YB + (cA) * 8 * XS_SLOT; \
      BSTORE4(wx, vcv, s1_); BSTORE4(wy, vcv, s2_); SB(); } while (0)
    SETUP_CONV(); SETUP_MAIN();
    WAITV(0); asm volatile("" :: "v"(dummy));
    { const unsigned so = w < 2 ? OFF_AF + (cg * 32 + rt) * 128 : 0xF0000000u; BSTORE2(tagv, v_one, so); }
    xstats();
    if (__builtin_expect(w < 2, 1)) {

    }
    BARRIER(); STUB(0, ".Lwret0"); GM();
    const unsigned vpa = (lane & 31) * 128, spa = OFF_AF + (b >> 4) * 4096;
    static_assert(U0S == 4288 && CV_UC == 4160, "literal offsets");
    v4u ua[2]; v2u ub[2];
    const unsigned su = OFF_UREV + d * (S * 2) + bh * 2048;
#define ULOADS() do { BLOAD4(ua[1], v_l16, su, 1024); asm volatile("buffer_load_dwordx2 %0, %1, %2, %3 offen offset:1040 sc1" : "=v"(ub[1]) : "v"(v_un), "s"(srd), "s"(su)); \
      BLOAD4(ua[0], v_l16, su, 0); asm volatile("buffer_load_dwordx2 %0, %1, %2, %3 offen offset:16 sc1" : "=v"(ub[0]) : "v"(v_l16), "s"(srd), "s"(su)); \
      ELOADS(bh, 3 - bh); } while (0)
    unsigned wa[4];
#pragma unroll
    for (int g = 0; g < 4; ++g) wa[g] = wa_l + ((g + w) & 3) * 2048;
    DSRTRW("=&v", wg[0], wa[0], 0, 512);
    DSRTRW("=&v", wg[7], wa[1], 0, 512);
    DSRTRW("=&v", wg[14], wa[2], 0, 512);
    DSRTRW("=&v", wg[21], wa[3], 0, 512);
    DSRTRW("=&v", wg[1], wa[0], 8192, 8704);
    DSRTRW("=&v", wg[8], wa[1], 8192, 8704);
    DSRTRW("=&v", wg[15], wa[2], 8192, 8704);
    WAITL(12); SB(); MF16B("v", csacc, sel, wg[0]); GM();
    DSRTRW("=&v", wg[22], wa[3], 8192, 8704);
    WAITL(12); SB(); MF16B("v", csacc, sel, wg[7]); TIEA(wg[7]); GM();
    DSRTRW("=&v", wg[2], wa[0], 16384, 16896);
    WAITL(12); SB(); MF16B("v", csacc, sel, wg[14]); TIEA(wg[14]); GM();
    DSRTRW("=&v", wg[9], wa[1], 16384, 16896);
    WAITL(12); SB(); MF16B("v", csacc, sel, wg[21]); TIEA(wg[21]); GM();
    DSRTRW("=&v", wg[16], wa[2], 16384, 16896);
    WAITL(12); SB(); MF16B("v", csacc, sel, wg[1]); GM();
    DSRTRW("=&v", wg[23], wa[3], 16384, 16896);
    WAITL(12); SB(); MF16B("v", csacc, sel, wg[8]); TIEA(wg[8]); GM();
    DSRTRW("=&v", wg[3], wa[0], 24576, 25088);
    WAITL(12); SB(); MF16B("v", csacc, sel, wg[15]); TIEA(wg[15]); GM();
    DSRTRW("=&v", wg[10], wa[1], 24576, 25088);
    WAITL(12); SB(); MF16B("v", csacc, sel, wg[22]); TIEA(wg[22]); GM();
    DSRTRW("=&v", wg[17], wa[2], 24576, 25088);
    WAITL(12); SB(); MF16B("v", csacc, sel, wg[2]); GM();
    DSRTRW("=&v", wg[24], wa[3], 24576, 25088);
    WAITL(12); SB(); MF16B("v", csacc, sel, wg[9]); TIEA(wg[9]); GM();
    DSRTRW("=&v", wg[4], wa[0], 32768, 33280);
    WAITL(12); SB(); MF16B("v", csacc, sel, wg[16]); TIEA(wg[16]); GM();
    DSRTRW("=&v", wg[11], wa[1], 32768, 33280);
    WAITL(12); SB(); MF16B("v", csacc, sel, wg[23]); TIEA(wg[23]); GM();
    DSRTRW("=&v", wg[18], wa[2], 32768, 33280);
    __builtin_amdgcn_s_sleep(FRONT_PACE1); BLOAD2(pollf, vpa, spa);
    WAITL(12); SB(); MF16B("v", csacc, sel, wg[3]); GM();
    DSRTRW("=&v", wg[25], wa[3], 32768, 33280);
    WAITL(12); SB(); MF16B("v", csacc, sel, wg[10]); TIEA(wg[10]); GM();
    DSRTRW("=&v", wg[5], wa[0], 40960, 41472);
    WAITL(12); SB(); MF16B("v", csacc, sel, wg[17]); TIEA(wg[17]); GM();
    DSRTRW("=&v", wg[12], wa[1], 40960, 41472);
    WAITL(12); SB(); MF16B("v", csacc, sel, wg[24]); TIEA(wg[24]); GM();
    DSRTRW("=&v", wg[19], wa[2], 40960, 41472);
    WAITL(12); SB(); MF16B("v", csacc, sel, wg[4]); GM();
    DSRTRW("=&v", wg[26], wa[3], 40960, 41472);
    WAITL(12); SB(); MF16B("v", csacc, sel, wg[11]); TIEA(wg[11]); GM();
    DSRTRW("=&v", wg[6], wa[0], 49152, 49664);
    WAITL(12); SB(); MF16B("v", csacc, sel, wg[18]); TIEA(wg[18]); GM();
    DSRTRW("=&v", wg[13], wa[1], 49152, 49664);
    WAITL(12); SB(); MF16B("v", csacc, sel, wg[25]); TIEA(wg[25]); GM();
    DSRTRW("=&v", wg[20], wa[2], 49152, 49664);
    WAITL(12); SB(); MF16B("v", csacc, sel, wg[5]); GM();
    DSRTRW("=&v", wg[27], wa[3], 49152, 49664);
    WAITL(12); SB(); MF16B("v", csacc, sel, wg[12]); TIEA(wg[12]); GM();
    WAITL(10); SB(); MF16B("v", csacc, sel, wg[19]); TIEA(wg[19]); GM();
    WAITL(8); SB(); MF16B("v", csacc, sel, wg[26]); TIEA(wg[26]); GM();
    WAITL(6); SB(); MF16B("v", csacc, sel, wg[6]); TIEA(wg[6]); GM();
    WAITL(4); SB(); MF16B("v", csacc, sel, wg[13]); TIEA(wg[13]); GM();
    WAITV(0); CHECKF(pollf, vpa, spa); ULOADS();
    WAITL(2); SB(); MF16B("v", csacc, sel, wg[20]); TIEA(wg[20]); GM();
    WAITL(0); SB(); MF16B("v", csacc, sel, wg[27]); TIEA(wg[27]); GM();
    NOPS();
    TIE(csacc);
    {
      const float csv = csacc[0], c0_ = rb(csv), c1_ = rb(csv - c0_), c2_ = csv - c0_ - c1_;
      aext = v4u{lane >= 32 ? pack2(c2_, 0.f) : pack2(c0_, c1_), 0u, 0u, 0u};
      TIE(aext);
    }
    HOP4(0, 1, 2, 3);
    HOP4(4, 5, 6, 7);
    HOP4(8, 9, 10, 11);
    HOP4(12, 13, 14, 15);
    HOP(16);
    {
      const v4u z4 = {0u, 0u, 0u, 0u};
      DSW128(cp_z, z4, 0);
#define CPY(c, o0, o1, o2, o3) { TIE2(ua[c], ub[c]); const unsigned a0 = ua[c][0], a1 = ua[c][1], a2 = ua[c][2], a3 = ua[c][3], a4 = ub[c][0], a5 = ub[c][1]; \
        DSW128(cp_rd, ua[c], o0); \
        const unsigned p1 = __builtin_amdgcn_alignbit(a2, a1, 16), p2 = __builtin_amdgcn_alignbit(a3, a2, 16), p3 = __builtin_amdgcn_alignbit(a4, a3, 16); \
        const v4u c1 = {__builtin_amdgcn_alignbit(a1, a0, 16), p1, p2, p3}, c2 = {a1, a2, a3, a4}, c3 = {p1, p2, p3, __builtin_amdgcn_alignbit(a5, a4, 16)}; \
        DSW128(cp_wr, c1, o1); DSW128(cp_wr, c2, o2); DSW128(cp_wr, c3, o3); }
      WAITV(8); SB();
      CPY(1, 1024, 1024, 5184, 9344)
      WAITV(6); SB();
      CPY(0, 0, 0, 4160, 8320)
      BLOAD2(pollx, v_ph, s_pw);
    }
    BARRIER(); GM();
    if (bh == 0) {
      MARK("ROLE w01");
      STUB(1, ".Lwret1"); GM();
      DSR128(Bk[0][3], Bb, 1984);
      DSR64(Al[0][0], Ab, 4032);
      DSR64(Ah[0][0], Ab, 4040);
      DSR128(Bk[0][0], Bb, 448);
      DSR128(Bk[0][2], Bb, 1472);
      DSR64(Al[0][1], Ab, 3520);
      DSR64(Ah[0][1], Ab, 3528);
      DSR128(Bk[0][1], Bb, 960);
      DSR64(Al[0][2], Ab, 3008);
      DSR64(Ah[0][2], Ab, 3016);
      DSR64(Al[0][3], Ab, 2496);
      DSR64(Ah[0][3], Ab, 2504);
      WAITL(9);
      MFC(1, ca1, Al[0][0], Ah[0][0], "v", Bk[0][3]); GM();
      DSR128(Bk[0][7], Bb, 1920);
      DSR64(Al[0][4], Ab, 3968);
      DSR64(Ah[0][4], Ab, 3976);
      WAITL(11);
      MFC(1, ca0, Al[0][0], Ah[0][0], "v", Bk[0][0]); GM();
      DSR128(Bk[0][4], Bb, 384);
      DSR128(Bk[0][6], Bb, 1408);
      WAITL(10);
      MFC(0, ca1, Al[0][1], Ah[0][1], "v", Bk[0][2]); GM();
      DSR64(Al[0][5], Ab, 3456);
      DSR64(Ah[0][5], Ab, 3464);
      DSR128(Bk[0][5], Bb, 896);
      WAITL(10);
      MFC(0, ca1, Al[0][2], Ah[0][2], "v", Bk[0][1]); GM();
      DSR64(Al[0][6], Ab, 2944);
      DSR64(Ah[0][6], Ab, 2952);
      WAITL(10);
      MFC(0, ca1, Al[0][3], Ah[0][3], "v", Bk[0][0]); GM();
      DSR64(Al[0][7], Ab, 2432);
      DSR64(Ah[0][7], Ab, 2440);
      DSR128(Bk[1][3], Bb, 1856);
      WAITL(10);
      MFC(0, ca1, Al[0][4], Ah[0][4], "v", Bk[0][7]); GM();
      DSR64(Al[1][0], Ab, 3904);
      DSR64(Ah[1][0], Ab, 3912);
      WAITL(11);
      MFC(0, ca0, Al[0][4], Ah[0][4], "v", Bk[0][4]); GM();
      DSR128(Bk[1][0], Bb, 320);
      DSR128(Bk[1][2], Bb, 1344);
      DSR64(Al[1][1], Ab, 3392);
      WAITL(11);
      MFC(0, ca1, Al[0][5], Ah[0][5], "v", Bk[0][6]); GM();
      DSR64(Ah[1][1], Ab, 3400);
      DSR128(Bk[1][1], Bb, 832);
      WAITL(10);
      MFC(0, ca1, Al[0][6], Ah[0][6], "v", Bk[0][5]); GM();
      DSR64(Al[1][2], Ab, 2880);
      DSR64(Ah[1][2], Ab, 2888);
      DSR64(Al[1][3], Ab, 2368);
      WAITL(11);
      MFC(0, ca1, Al[0][7], Ah[0][7], "v", Bk[0][4]); GM();
      DSR64(Ah[1][3], Ab, 2376);
      DSR128(Bk[1][7], Bb, 1792);
      WAITL(10);
      MFC(0, ca1, Al[1][0], Ah[1][0], "v", Bk[1][3]); GM();
      DSR64(Al[1][4], Ab, 3840);
      DSR64(Ah[1][4], Ab, 3848);
      DSR128(Bk[1][4], Bb, 256);
      WAITL(12);
      MFC(0, ca0, Al[1][0], Ah[1][0], "v", Bk[1][0]); GM();
      DSR128(Bk[1][6], Bb, 1280);
      DSR64(Al[1][5], Ab, 3328);
      WAITL(11);
      MFC(0, ca1, Al[1][1], Ah[1][1], "v", Bk[1][2]); GM();
      DSR64(Ah[1][5], Ab, 3336);
      DSR128(Bk[1][5], Bb, 768);
      DSR64(Al[1][6], Ab, 2816);
      WAITL(11);
      MFC(0, ca1, Al[1][2], Ah[1][2], "v", Bk[1][1]); GM();
      DSR64(Ah[1][6], Ab, 2824);
      DSR64(Al[1][7], Ab, 2304);
      WAITL(11);
      MFC(0, ca1, Al[1][3], Ah[1][3], "v", Bk[1][0]); GM();
      DSR64(Ah[1][7], Ab, 2312);
      DSR128(Bk[0][3], Bb, 1728);
      DSR64(Al[0][0], Ab, 3776);
      WAITL(11);
      MFC(0, ca1, Al[1][4], Ah[1][4], "v", Bk[1][7]); GM();
      DSR64(Ah[0][0], Ab, 3784);
      DSR128(Bk[0][0], Bb, 192);
      WAITL(12);
      MFC(0, ca0, Al[1][4], Ah[1][4], "v", Bk[1][4]); GM();
      DSR128(Bk[0][2], Bb, 1216);
      DSR64(Al[0][1], Ab, 3264);
      DSR64(Ah[0][1], Ab, 3272);
      WAITL(12);
      MFC(0, ca1, Al[1][5], Ah[1][5], "v", Bk[1][6]); GM();
      DSR128(Bk[0][1], Bb, 704);
      DSR64(Al[0][2], Ab, 2752);
      WAITL(11);
      MFC(0, ca1, Al[1][6], Ah[1][6], "v", Bk[1][5]); GM();
      DSR64(Ah[0][2], Ab, 2760);
      DSR64(Al[0][3], Ab, 2240);
      DSR64(Ah[0][3], Ab, 2248);
      WAITL(12);
      MFC(0, ca1, Al[1][7], Ah[1][7], "v", Bk[1][4]); GM();
      DSR128(Bk[0][7], Bb, 1664);
      DSR64(Al[0][4], Ab, 3712);
      WAITL(11);
      MFC(0, ca1, Al[0][0], Ah[0][0], "v", Bk[0][3]); GM();
      DSR64(Ah[0][4], Ab, 3720);
      DSR128(Bk[0][4], Bb, 128);
      DSR128(Bk[0][6], Bb, 1152);
      WAITL(13);
      MFC(0, ca0, Al[0][0], Ah[0][0], "v", Bk[0][0]); GM();
      DSR64(Al[0][5], Ab, 3200);
      DSR64(Ah[0][5], Ab, 3208);
      WAITL(12);
      MFC(0, ca1, Al[0][1], Ah[0][1], "v", Bk[0][2]); GM();
      DSR128(Bk[0][5], Bb, 640);
      DSR64(Al[0][6], Ab, 2688);
      DSR64(Ah[0][6], Ab, 2696);
      WAITL(12);
      MFC(0, ca1, Al[0][2], Ah[0][2], "v", Bk[0][1]); GM();
      DSR64(Al[0][7], Ab, 2176);
      DSR64(Ah[0][7], Ab, 2184);
      WAITL(12);
      MFC(0, ca1, Al[0][3], Ah[0][3], "v", Bk[0][0]); GM();
      DSR128(Bk[1][3], Bb, 1600);
      DSR64(Al[1][0], Ab, 3648);
      DSR64(Ah[1][0], Ab, 3656);
      WAITL(12);
      MFC(0, ca1, Al[0][4], Ah[0][4], "v", Bk[0][7]); GM();
      DSR128(Bk[1][0], Bb, 64);
      DSR128(Bk[1][2], Bb, 1088);
      WAITL(13);
      MFC(0, ca0, Al[0][4], Ah[0][4], "v", Bk[0][4]); GM();
      DSR64(Al[1][1], Ab, 3136);
      DSR64(Ah[1][1], Ab, 3144);
      DSR128(Bk[1][1], Bb, 576);
      WAITL(13);
      MFC(0, ca1, Al[0][5], Ah[0][5], "v", Bk[0][6]); GM();
      DSR64(Al[1][2], Ab, 2624);
      DSR64(Ah[1][2], Ab, 2632);
      WAITL(12);
      MFC(0, ca1, Al[0][6], Ah[0][6], "v", Bk[0][5]); GM();
      DSR64(Al[1][3], Ab, 2112);
      DSR64(Ah[1][3], Ab, 2120);
      DSR128(Bk[1][7], Bb, 1536);
      WAITL(13);
      MFC(0, ca1, Al[0][7], Ah[0][7], "v", Bk[0][4]); GM();
      DSR64(Al[1][4], Ab, 3584);
      DSR64(Ah[1][4], Ab, 3592);
      WAITL(12);
      MFC(0, ca1, Al[1][0], Ah[1][0], "v", Bk[1][3]); GM();
      DSR128(Bk[1][4], Bb, 0);
      DSR128(Bk[1][6], Bb, 1024);
      DSR64(Al[1][5], Ab, 3072);
      WAITL(14);
      MFC(0, ca0, Al[1][0], Ah[1][0], "v", Bk[1][0]); GM();
      DSR64(Ah[1][5], Ab, 3080);
      DSR128(Bk[1][5], Bb, 512);
      WAITL(13);
      MFC(0, ca1, Al[1][1], Ah[1][1], "v", Bk[1][2]); GM();
      DSR64(Al[1][6], Ab, 2560);
      DSR64(Ah[1][6], Ab, 2568);
      DSR64(Al[1][7], Ab, 2048);
      WAITL(13);
      MFC(0, ca1, Al[1][2], Ah[1][2], "v", Bk[1][1]); GM();
      DSR64(Ah[1][7], Ab, 2056);
      DSR64(ur[0], Ur, 3584);
      WAITL(13);
      MFC(0, ca1, Al[1][3], Ah[1][3], "v", Bk[1][0]); GM();
      DSR64(ur[1], Ur, 2048);
      WAITL(11);
      MFC(0, ca1, Al[1][4], Ah[1][4], "v", Bk[1][7]); GM();
      WAITL(10);
      MFC(0, ca0, Al[1][4], Ah[1][4], "v", Bk[1][4]); GM();
      WAITL(7);
      MFC(0, ca1, Al[1][5], Ah[1][5], "v", Bk[1][6]); GM();
      WAITL(4);
      MFC(0, ca1, Al[1][6], Ah[1][6], "v", Bk[1][5]); GM();
      WAITL(2);
      MFC(0, ca1, Al[1][7], Ah[1][7], "v", Bk[1][4]); GM();
    } else {
      MARK("ROLE w23");
      STUB(2, ".Lwret2"); GM();
      DSR128(Bk[0][2], Bb, 1472);
      DSR64(Al[0][0], Ab, 4032);
      DSR64(Ah[0][0], Ab, 4040);
      DSR128(Bk[0][1], Bb, 960);
      DSR64(Al[0][1], Ab, 3520);
      DSR64(Ah[0][1], Ab, 3528);
      DSR128(Bk[0][0], Bb, 448);
      DSR64(Al[0][2], Ab, 3008);
      DSR64(Ah[0][2], Ab, 3016);
      DSR128(Bk[0][6], Bb, 1408);
      DSR64(Al[0][4], Ab, 3968);
      DSR64(Ah[0][4], Ab, 3976);
      WAITL(9);
      MFC(1, ca1, Al[0][0], Ah[0][0], "v", Bk[0][2]); GM();
      DSR128(Bk[0][5], Bb, 896);
      DSR64(Al[0][5], Ab, 3456);
      DSR64(Ah[0][5], Ab, 3464);
      WAITL(11);
      MFC(1, ca0, Al[0][0], Ah[0][0], "v", Bk[0][1]); GM();
      DSR128(Bk[0][4], Bb, 384);
      DSR64(Al[0][6], Ab, 2944);
      WAITL(11);
      MFC(0, ca1, Al[0][1], Ah[0][1], "v", Bk[0][1]); GM();
      DSR64(Ah[0][6], Ab, 2952);
      DSR128(Bk[1][2], Bb, 1344);
      DSR64(Al[1][0], Ab, 3904);
      WAITL(13);
      MFC(0, ca0, Al[0][1], Ah[0][1], "v", Bk[0][0]); GM();
      DSR64(Ah[1][0], Ab, 3912);
      DSR128(Bk[1][1], Bb, 832);
      WAITL(13);
      MFC(0, ca1, Al[0][2], Ah[0][2], "v", Bk[0][0]); GM();
      DSR64(Al[1][1], Ab, 3392);
      DSR64(Ah[1][1], Ab, 3400);
      DSR128(Bk[1][0], Bb, 320);
      WAITL(13);
      MFC(0, ca1, Al[0][4], Ah[0][4], "v", Bk[0][6]); GM();
      DSR64(Al[1][2], Ab, 2880);
      DSR64(Ah[1][2], Ab, 2888);
      WAITL(14);
      MFC(0, ca0, Al[0][4], Ah[0][4], "v", Bk[0][5]); GM();
      DSR128(Bk[1][6], Bb, 1280);
      DSR64(Al[1][4], Ab, 3840);
      DSR64(Ah[1][4], Ab, 3848);
      WAITL(14);
      MFC(0, ca1, Al[0][5], Ah[0][5], "v", Bk[0][5]); GM();
      DSR128(Bk[1][5], Bb, 768);
      DSR64(Al[1][5], Ab, 3328);
      MFC(0, ca0, Al[0][5], Ah[0][5], "v", Bk[0][4]); GM();
      DSR64(Ah[1][5], Ab, 3336);
      DSR128(Bk[1][4], Bb, 256);
      DSR64(Al[1][6], Ab, 2816);
      WAITL(14);
      MFC(0, ca1, Al[0][6], Ah[0][6], "v", Bk[0][4]); GM();
      DSR64(Ah[1][6], Ab, 2824);
      DSR128(Bk[0][2], Bb, 1216);
      MFC(0, ca1, Al[1][0], Ah[1][0], "v", Bk[1][2]); GM();
      DSR64(Al[0][0], Ab, 3776);
      DSR64(Ah[0][0], Ab, 3784);
      DSR128(Bk[0][1], Bb, 704);
      WAITL(14);
      MFC(0, ca0, Al[1][0], Ah[1][0], "v", Bk[1][1]); GM();
      DSR64(Al[0][1], Ab, 3264);
      DSR64(Ah[0][1], Ab, 3272);
      MFC(0, ca1, Al[1][1], Ah[1][1], "v", Bk[1][1]); GM();
      DSR128(Bk[0][0], Bb, 192);
      DSR64(Al[0][2], Ab, 2752);
      DSR64(Ah[0][2], Ab, 2760);
      MFC(0, ca0, Al[1][1], Ah[1][1], "v", Bk[1][0]); GM();
      DSR128(Bk[0][6], Bb, 1152);
      DSR64(Al[0][4], Ab, 3712);
      WAITL(14);
      MFC(0, ca1, Al[1][2], Ah[1][2], "v", Bk[1][0]); GM();
      DSR64(Ah[0][4], Ab, 3720);
      DSR128(Bk[0][5], Bb, 640);
      DSR64(Al[0][5], Ab, 3200);
      MFC(0, ca1, Al[1][4], Ah[1][4], "v", Bk[1][6]); GM();
      DSR64(Ah[0][5], Ab, 3208);
      DSR128(Bk[0][4], Bb, 128);
      MFC(0, ca0, Al[1][4], Ah[1][4], "v", Bk[1][5]); GM();
      DSR64(Al[0][6], Ab, 2688);
      DSR64(Ah[0][6], Ab, 2696);
      DSR128(Bk[1][2], Bb, 1088);
      MFC(0, ca1, Al[1][5], Ah[1][5], "v", Bk[1][5]); GM();
      DSR64(Al[1][0], Ab, 3648);
      DSR64(Ah[1][0], Ab, 3656);
      WAITL(14);
      MFC(0, ca0, Al[1][5], Ah[1][5], "v", Bk[1][4]); GM();
      DSR128(Bk[1][1], Bb, 576);
      DSR64(Al[1][1], Ab, 3136);
      DSR64(Ah[1][1], Ab, 3144);
      MFC(0, ca1, Al[1][6], Ah[1][6], "v", Bk[1][4]); GM();
      DSR128(Bk[1][0], Bb, 64);
      DSR64(Al[1][2], Ab, 2624);
      MFC(0, ca1, Al[0][0], Ah[0][0], "v", Bk[0][2]); GM();
      DSR64(Ah[1][2], Ab, 2632);
      DSR128(Bk[1][6], Bb, 1024);
      DSR64(Al[1][4], Ab, 3584);
      MFC(0, ca0, Al[0][0], Ah[0][0], "v", Bk[0][1]); GM();
      DSR64(Ah[1][4], Ab, 3592);
      DSR128(Bk[1][5], Bb, 512);
      MFC(0, ca1, Al[0][1], Ah[0][1], "v", Bk[0][1]); GM();
      DSR64(Al[1][5], Ab, 3072);
      DSR64(Ah[1][5], Ab, 3080);
      DSR128(Bk[1][4], Bb, 0);
      MFC(0, ca0, Al[0][1], Ah[0][1], "v", Bk[0][0]); GM();
      DSR64(Al[1][6], Ab, 2560);
      DSR64(Ah[1][6], Ab, 2568);
      WAITL(14);
      MFC(0, ca1, Al[0][2], Ah[0][2], "v", Bk[0][0]); GM();
      MFC(0, ca1, Al[0][4], Ah[0][4], "v", Bk[0][6]); GM();
      MFC(0, ca0, Al[0][4], Ah[0][4], "v", Bk[0][5]); GM();
      MFC(0, ca1, Al[0][5], Ah[0][5], "v", Bk[0][5]); GM();
      MFC(0, ca0, Al[0][5], Ah[0][5], "v", Bk[0][4]); GM();
      MFC(0, ca1, Al[0][6], Ah[0][6], "v", Bk[0][4]); GM();
      MFC(0, ca1, Al[1][0], Ah[1][0], "v", Bk[1][2]); GM();
      DSR64(ur[0], Ur, 3072);
      DSR64(ur[1], Ur, 2560);
      MFC(0, ca0, Al[1][0], Ah[1][0], "v", Bk[1][1]); GM();
      WAITL(14);
      MFC(0, ca1, Al[1][1], Ah[1][1], "v", Bk[1][1]); GM();
      WAITL(13);
      MFC(0, ca0, Al[1][1], Ah[1][1], "v", Bk[1][0]); GM();
      WAITL(11);
      MFC(0, ca1, Al[1][2], Ah[1][2], "v", Bk[1][0]); GM();
      WAITL(8);
      MFC(0, ca1, Al[1][4], Ah[1][4], "v", Bk[1][6]); GM();
      WAITL(7);
      MFC(0, ca0, Al[1][4], Ah[1][4], "v", Bk[1][5]); GM();
      WAITL(5);
      MFC(0, ca1, Al[1][5], Ah[1][5], "v", Bk[1][5]); GM();
      WAITL(4);
      MFC(0, ca0, Al[1][5], Ah[1][5], "v", Bk[1][4]); GM();
      WAITL(2);
      MFC(0, ca1, Al[1][6], Ah[1][6], "v", Bk[1][4]); GM();
    }
    WAITV(0); WAITL(0); NOPS(); SB(); STUB(3, ".Lwret3"); GM();
    CONV_EPI(bh, 3 - bh, v_cv, ca0, ca1, ur[0], ur[1]); GM();
    ELOADS(4 + bh, 7 - bh);
#define FLAG0() do { SBAR(); BSTORE2(tagv, v_one, sCf0); CHECKF(pollx, v_ph, s_pw); } while (0)
    if (bh == 0) {
      MARK("ROLE w01");
      STUB(4, ".Lwret4"); GM();
      DSR128(Bk[0][7], Bb, 4032);
      DSR64(Al[0][0], Ab, 4032);
      DSR64(Ah[0][0], Ab, 4040);
      DSR128(Bk[0][4], Bb, 2496);
      DSR128(Bk[0][6], Bb, 3520);
      DSR64(Al[0][1], Ab, 3520);
      DSR64(Ah[0][1], Ab, 3528);
      DSR128(Bk[0][3], Bb, 1984);
      DSR128(Bk[0][5], Bb, 3008);
      DSR64(Al[0][2], Ab, 3008);
      DSR64(Ah[0][2], Ab, 3016);
      DSR128(Bk[0][2], Bb, 1472);
      WAITL(9);
      MFC(1, ca1, Al[0][0], Ah[0][0], "v", Bk[0][7]); GM();
      DSR64(Al[0][3], Ab, 2496);
      DSR64(Ah[0][3], Ab, 2504);
      WAITL(10);
      MFC(1, ca0, Al[0][0], Ah[0][0], "v", Bk[0][4]); GM();
      DSR128(Bk[0][1], Bb, 960);
      DSR64(Al[0][4], Ab, 1984);
      WAITL(9);
      MFC(0, ca1, Al[0][1], Ah[0][1], "v", Bk[0][6]); GM();
      DSR64(Ah[0][4], Ab, 1992);
      DSR128(Bk[0][0], Bb, 448);
      WAITL(10);
      MFC(0, ca0, Al[0][1], Ah[0][1], "v", Bk[0][3]); GM();
      DSR64(Al[0][5], Ab, 1472);
      DSR64(Ah[0][5], Ab, 1480);
      WAITL(9);
      MFC(0, ca1, Al[0][2], Ah[0][2], "v", Bk[0][5]); GM();
      DSR64(Al[0][6], Ab, 960);
      DSR64(Ah[0][6], Ab, 968);
      WAITL(10);
      MFC(0, ca0, Al[0][2], Ah[0][2], "v", Bk[0][2]); GM();
      DSR64(Al[0][7], Ab, 448);
      DSR64(Ah[0][7], Ab, 456);
      WAITL(10);
      MFC(0, ca1, Al[0][3], Ah[0][3], "v", Bk[0][4]); GM();
      DSR128(Bk[1][7], Bb, 3968);
      DSR64(Al[1][0], Ab, 3968);
      WAITL(11);
      MFC(0, ca0, Al[0][3], Ah[0][3], "v", Bk[0][1]); GM();
      DSR64(Ah[1][0], Ab, 3976);
      DSR128(Bk[1][4], Bb, 2432);
      WAITL(11);
      MFC(0, ca1, Al[0][4], Ah[0][4], "v", Bk[0][3]); GM();
      DSR128(Bk[1][6], Bb, 3456);
      DSR64(Al[1][1], Ab, 3456);
      WAITL(12);
      MFC(0, ca0, Al[0][4], Ah[0][4], "v", Bk[0][0]); GM();
      DSR64(Ah[1][1], Ab, 3464);
      DSR128(Bk[1][3], Bb, 1920);
      WAITL(12);
      MFC(0, ca1, Al[0][5], Ah[0][5], "v", Bk[0][2]); GM();
      DSR128(Bk[1][5], Bb, 2944);
      DSR64(Al[1][2], Ab, 2944);
      WAITL(12);
      MFC(0, ca1, Al[0][6], Ah[0][6], "v", Bk[0][1]); GM();
      DSR64(Ah[1][2], Ab, 2952);
      DSR128(Bk[1][2], Bb, 1408);
      WAITL(12);
      MFC(0, ca1, Al[0][7], Ah[0][7], "v", Bk[0][0]); GM();
      DSR64(Al[1][3], Ab, 2432);
      DSR64(Ah[1][3], Ab, 2440);
      WAITL(11);
      MFC(0, ca1, Al[1][0], Ah[1][0], "v", Bk[1][7]); GM();
      DSR128(Bk[1][1], Bb, 896);
      DSR64(Al[1][4], Ab, 1920);
      WAITL(12);
      MFC(0, ca0, Al[1][0], Ah[1][0], "v", Bk[1][4]); GM();
      DSR64(Ah[1][4], Ab, 1928);
      DSR128(Bk[1][0], Bb, 384);
      WAITL(11);
      MFC(0, ca1, Al[1][1], Ah[1][1], "v", Bk[1][6]); GM();
      DSR64(Al[1][5], Ab, 1408);
      DSR64(Ah[1][5], Ab, 1416);
      WAITL(12);
      MFC(0, ca0, Al[1][1], Ah[1][1], "v", Bk[1][3]); GM();
      DSR64(Al[1][6], Ab, 896);
      DSR64(Ah[1][6], Ab, 904);
      WAITL(11);
      MFC(0, ca1, Al[1][2], Ah[1][2], "v", Bk[1][5]); GM();
      DSR64(Al[1][7], Ab, 384);
      DSR64(Ah[1][7], Ab, 392);
      WAITL(12);
      MFC(0, ca0, Al[1][2], Ah[1][2], "v", Bk[1][2]); GM();
      DSR128(Bk[0][7], Bb, 3904);
      DSR64(Al[0][0], Ab, 3904);
      WAITL(12);
      MFC(0, ca1, Al[1][3], Ah[1][3], "v", Bk[1][4]); GM();
      DSR64(Ah[0][0], Ab, 3912);
      DSR128(Bk[0][4], Bb, 2368);
      WAITL(13);
      MFC(0, ca0, Al[1][3], Ah[1][3], "v", Bk[1][1]); GM();
      DSR128(Bk[0][6], Bb, 3392);
      DSR64(Al[0][1], Ab, 3392);
      WAITL(13);
      MFC(0, ca1, Al[1][4], Ah[1][4], "v", Bk[1][3]); GM();
      DSR64(Ah[0][1], Ab, 3400);
      DSR128(Bk[0][3], Bb, 1856);
      WAITL(14);
      MFC(0, ca0, Al[1][4], Ah[1][4], "v", Bk[1][0]); GM();
      DSR128(Bk[0][5], Bb, 2880);
      DSR64(Al[0][2], Ab, 2880);
      WAITV(6); FLAG0();
      WAITL(14);
      MFC(0, ca1, Al[1][5], Ah[1][5], "v", Bk[1][2]); GM();
      DSR64(Ah[0][2], Ab, 2888);
      DSR128(Bk[0][2], Bb, 1344);
      BLOADWD(wd[0][0], 0);
      WAITL(14);
      MFC(0, ca1, Al[1][6], Ah[1][6], "v", Bk[1][1]); GM();
      DSR64(Al[0][3], Ab, 2368);
      DSR64(Ah[0][3], Ab, 2376);
      WAITL(14);
      MFC(0, ca1, Al[1][7], Ah[1][7], "v", Bk[1][0]); GM();
      DSR128(Bk[0][1], Bb, 832);
      DSR64(Al[0][4], Ab, 1856);
      BLOADWD(wd[0][1], 57344);
      WAITL(13);
      MFC(0, ca1, Al[0][0], Ah[0][0], "v", Bk[0][7]); GM();
      DSR64(Ah[0][4], Ab, 1864);
      DSR128(Bk[0][0], Bb, 320);
      WAITL(14);
      MFC(0, ca0, Al[0][0], Ah[0][0], "v", Bk[0][4]); GM();
      DSR64(Al[0][5], Ab, 1344);
      DSR64(Ah[0][5], Ab, 1352);
      BLOADWD(wd[0][2], 114688);
      WAITL(13);
      MFC(0, ca1, Al[0][1], Ah[0][1], "v", Bk[0][6]); GM();
      DSR64(Al[0][6], Ab, 832);
      DSR64(Ah[0][6], Ab, 840);
      WAITL(14);
      MFC(0, ca0, Al[0][1], Ah[0][1], "v", Bk[0][3]); GM();
      DSR64(Al[0][7], Ab, 320);
      DSR64(Ah[0][7], Ab, 328);
      BLOADWD(wd[1][0], 1024);
      WAITL(13);
      MFC(0, ca1, Al[0][2], Ah[0][2], "v", Bk[0][5]); GM();
      DSR128(Bk[1][7], Bb, 3840);
      DSR64(Al[1][0], Ab, 3840);
      WAITL(14);
      MFC(0, ca0, Al[0][2], Ah[0][2], "v", Bk[0][2]); GM();
      DSR64(Ah[1][0], Ab, 3848);
      DSR128(Bk[1][4], Bb, 2304);
      BLOADWD(wd[1][1], 58368);
      WAITL(14);
      MFC(0, ca1, Al[0][3], Ah[0][3], "v", Bk[0][4]); GM();
      DSR128(Bk[1][6], Bb, 3328);
      DSR64(Al[1][1], Ab, 3328);
      WAITL(14);
      MFC(0, ca0, Al[0][3], Ah[0][3], "v", Bk[0][1]); GM();
      DSR64(Ah[1][1], Ab, 3336);
      DSR128(Bk[1][3], Bb, 1792);
      BLOADWD(wd[1][2], 115712);
      WAITL(14);
      MFC(0, ca1, Al[0][4], Ah[0][4], "v", Bk[0][3]); GM();
      DSR128(Bk[1][5], Bb, 2816);
      DSR64(Al[1][2], Ab, 2816);
      MFC(0, ca0, Al[0][4], Ah[0][4], "v", Bk[0][0]); GM();
      DSR64(Ah[1][2], Ab, 2824);
      DSR128(Bk[1][2], Bb, 1280);
      BLOADWD(wd[2][0], 2048);
      WAITL(14);
      MFC(0, ca1, Al[0][5], Ah[0][5], "v", Bk[0][2]); GM();
      DSR64(Al[1][3], Ab, 2304);
      DSR64(Ah[1][3], Ab, 2312);
      MFC(0, ca1, Al[0][6], Ah[0][6], "v", Bk[0][1]); GM();
      DSR128(Bk[1][1], Bb, 768);
      DSR64(Al[1][4], Ab, 1792);
      BLOADWD(wd[2][1], 59392);
      WAITL(14);
      MFC(0, ca1, Al[0][7], Ah[0][7], "v", Bk[0][0]); STUB(5, ".Lwret5"); GM();
      DSR64(Ah[1][4], Ab, 1800);
      DSR128(Bk[1][0], Bb, 256);
      WAITL(14);
      MFC(0, ca1, Al[1][0], Ah[1][0], "v", Bk[1][7]); GM();
      DSR64(Al[1][5], Ab, 1280);
      DSR64(Ah[1][5], Ab, 1288);
      BLOADWD(wd[2][2], 116736);
      MFC(0, ca0, Al[1][0], Ah[1][0], "v", Bk[1][4]); GM();
      DSR64(Al[1][6], Ab, 768);
      DSR64(Ah[1][6], Ab, 776);
      WAITL(14);
      MFC(0, ca1, Al[1][1], Ah[1][1], "v", Bk[1][6]); GM();
      DSR64(Al[1][7], Ab, 256);
      DSR64(Ah[1][7], Ab, 264);
      BLOADWD(wd[3][0], 3072);
      MFC(0, ca0, Al[1][1], Ah[1][1], "v", Bk[1][3]); GM();
      DSR128(Bk[0][7], Bb, 3776);
      DSR64(Al[0][0], Ab, 3776);
      WAITL(14);
      MFC(0, ca1, Al[1][2], Ah[1][2], "v", Bk[1][5]); GM();
      DSR64(Ah[0][0], Ab, 3784);
      DSR128(Bk[0][4], Bb, 2240);
      BLOADWD(wd[3][1], 60416);
      MFC(0, ca0, Al[1][2], Ah[1][2], "v", Bk[1][2]); GM();
      DSR128(Bk[0][6], Bb, 3264);
      DSR64(Al[0][1], Ab, 3264);
      WAITL(14);
      MFC(0, ca1, Al[1][3], Ah[1][3], "v", Bk[1][4]); GM();
      DSR64(Ah[0][1], Ab, 3272);
      DSR128(Bk[0][3], Bb, 1728);
      BLOADWD(wd[3][2], 117760);
      MFC(0, ca0, Al[1][3], Ah[1][3], "v", Bk[1][1]); GM();
      DSR128(Bk[0][5], Bb, 2752);
      DSR64(Al[0][2], Ab, 2752);
      WAITL(14);
      MFC(0, ca1, Al[1][4], Ah[1][4], "v", Bk[1][3]); GM();
      DSR64(Ah[0][2], Ab, 2760);
      DSR128(Bk[0][2], Bb, 1216);
      BLOADWD(wd[4][0], 4096);
      MFC(0, ca0, Al[1][4], Ah[1][4], "v", Bk[1][0]); GM();
      DSR64(Al[0][3], Ab, 2240);
      DSR64(Ah[0][3], Ab, 2248);
      MFC(0, ca1, Al[1][5], Ah[1][5], "v", Bk[1][2]); GM();
      DSR128(Bk[0][1], Bb, 704);
      DSR64(Al[0][4], Ab, 1728);
      BLOADWD(wd[4][1], 61440);
      WAITL(14);
      MFC(0, ca1, Al[1][6], Ah[1][6], "v", Bk[1][1]); GM();
      DSR64(Ah[0][4], Ab, 1736);
      DSR128(Bk[0][0], Bb, 192);
      MFC(0, ca1, Al[1][7], Ah[1][7], "v", Bk[1][0]); GM();
      DSR64(Al[0][5], Ab, 1216);
      DSR64(Ah[0][5], Ab, 1224);
      BLOADWD(wd[4][2], 118784);
      WAITL(14);
      MFC(0, ca1, Al[0][0], Ah[0][0], "v", Bk[0][7]); GM();
      DSR64(Al[0][6], Ab, 704);
      DSR64(Ah[0][6], Ab, 712);
      MFC(0, ca0, Al[0][0], Ah[0][0], "v", Bk[0][4]); GM();
      DSR64(Al[0][7], Ab, 192);
      DSR64(Ah[0][7], Ab, 200);
      BLOADWD(wd[5][0], 5120);
      WAITL(14);
      MFC(0, ca1, Al[0][1], Ah[0][1], "v", Bk[0][6]); GM();
      DSR128(Bk[1][7], Bb, 3712);
      DSR64(Al[1][0], Ab, 3712);
      MFC(0, ca0, Al[0][1], Ah[0][1], "v", Bk[0][3]); GM();
      DSR64(Ah[1][0], Ab, 3720);
      DSR128(Bk[1][4], Bb, 2176);
      BLOADWD(wd[5][1], 62464);
      WAITL(14);
      MFC(0, ca1, Al[0][2], Ah[0][2], "v", Bk[0][5]); GM();
      DSR128(Bk[1][6], Bb, 3200);
      DSR64(Al[1][1], Ab, 3200);
      MFC(0, ca0, Al[0][2], Ah[0][2], "v", Bk[0][2]); GM();
      DSR64(Ah[1][1], Ab, 3208);
      DSR128(Bk[1][3], Bb, 1664);
      BLOADWD(wd[5][2], 119808);
      MFC(0, ca1, Al[0][3], Ah[0][3], "v", Bk[0][4]); GM();
      DSR128(Bk[1][5], Bb, 2688);
      DSR64(Al[1][2], Ab, 2688);
      WAITL(14);
      MFC(0, ca0, Al[0][3], Ah[0][3], "v", Bk[0][1]); GM();
      DSR64(Ah[1][2], Ab, 2696);
      DSR128(Bk[1][2], Bb, 1152);
      BLOADWD(wd[6][0], 6144);
      MFC(0, ca1, Al[0][4], Ah[0][4], "v", Bk[0][3]); GM();
      DSR64(Al[1][3], Ab, 2176);
      DSR64(Ah[1][3], Ab, 2184);
      MFC(0, ca0, Al[0][4], Ah[0][4], "v", Bk[0][0]); GM();
      DSR128(Bk[1][1], Bb, 640);
      DSR64(Al[1][4], Ab, 1664);
      BLOADWD(wd[6][1], 63488);
      MFC(0, ca1, Al[0][5], Ah[0][5], "v", Bk[0][2]); GM();
      DSR64(Ah[1][4], Ab, 1672);
      DSR128(Bk[1][0], Bb, 128);
      WAITL(14);
      MFC(0, ca1, Al[0][6], Ah[0][6], "v", Bk[0][1]); GM();
      DSR64(Al[1][5], Ab, 1152);
      DSR64(Ah[1][5], Ab, 1160);
      BLOADWD(wd[6][2], 120832);
      MFC(0, ca1, Al[0][7], Ah[0][7], "v", Bk[0][0]); GM();
      DSR64(Al[1][6], Ab, 640);
      DSR64(Ah[1][6], Ab, 648);
      MFC(0, ca1, Al[1][0], Ah[1][0], "v", Bk[1][7]); GM();
      DSR64(Al[1][7], Ab, 128);
      DSR64(Ah[1][7], Ab, 136);
      BLOADWD(wd[7][0], 7168);
      MFC(0, ca0, Al[1][0], Ah[1][0], "v", Bk[1][4]); GM();
      DSR128(Bk[0][7], Bb, 3648);
      DSR64(Al[0][0], Ab, 3648);
      WAITL(14);
      MFC(0, ca1, Al[1][1], Ah[1][1], "v", Bk[1][6]); GM();
      DSR64(Ah[0][0], Ab, 3656);
      DSR128(Bk[0][4], Bb, 2112);
      BLOADWD(wd[7][1], 64512);
      MFC(0, ca0, Al[1][1], Ah[1][1], "v", Bk[1][3]); GM();
      DSR128(Bk[0][6], Bb, 3136);
      DSR64(Al[0][1], Ab, 3136);
      MFC(0, ca1, Al[1][2], Ah[1][2], "v", Bk[1][5]); GM();
      DSR64(Ah[0][1], Ab, 3144);
      DSR128(Bk[0][3], Bb, 1600);
      BLOADWD(wd[7][2], 121856);
      MFC(0, ca0, Al[1][2], Ah[1][2], "v", Bk[1][2]); GM();
      DSR128(Bk[0][5], Bb, 2624);
      DSR64(Al[0][2], Ab, 2624);
      WAITL(14);
      MFC(0, ca1, Al[1][3], Ah[1][3], "v", Bk[1][4]); GM();
      DSR64(Ah[0][2], Ab, 2632);
      DSR128(Bk[0][2], Bb, 1088);
      MFC(0, ca0, Al[1][3], Ah[1][3], "v", Bk[1][1]); GM();
      DSR64(Al[0][3], Ab, 2112);
      DSR64(Ah[0][3], Ab, 2120);
      MFC(0, ca1, Al[1][4], Ah[1][4], "v", Bk[1][3]); GM();
      DSR128(Bk[0][1], Bb, 576);
      DSR64(Al[0][4], Ab, 1600);
      BLOAD2(pollh, v_pc, sCp);
      MFC(0, ca0, Al[1][4], Ah[1][4], "v", Bk[1][0]); GM();
      DSR64(Ah[0][4], Ab, 1608);
      DSR128(Bk[0][0], Bb, 64);
      MFC(0, ca1, Al[1][5], Ah[1][5], "v", Bk[1][2]); GM();
      DSR64(Al[0][5], Ab, 1088);
      DSR64(Ah[0][5], Ab, 1096);
      WAITL(14);
      MFC(0, ca1, Al[1][6], Ah[1][6], "v", Bk[1][1]); GM();
      DSR64(Al[0][6], Ab, 576);
      DSR64(Ah[0][6], Ab, 584);
      MFC(0, ca1, Al[1][7], Ah[1][7], "v", Bk[1][0]); GM();
      DSR64(Al[0][7], Ab, 64);
      DSR64(Ah[0][7], Ab, 72);
      MFC(0, ca1, Al[0][0], Ah[0][0], "v", Bk[0][7]); GM();
      DSR128(Bk[1][7], Bb, 3584);
      DSR64(Al[1][0], Ab, 3584);
      MFC(0, ca0, Al[0][0], Ah[0][0], "v", Bk[0][4]); GM();
      DSR64(Ah[1][0], Ab, 3592);
      DSR128(Bk[1][4], Bb, 2048);
      WAITL(14);
      MFC(0, ca1, Al[0][1], Ah[0][1], "v", Bk[0][6]); GM();
      DSR128(Bk[1][6], Bb, 3072);
      DSR64(Al[1][1], Ab, 3072);
      MFC(0, ca0, Al[0][1], Ah[0][1], "v", Bk[0][3]); GM();
      DSR64(Ah[1][1], Ab, 3080);
      DSR128(Bk[1][3], Bb, 1536);
      MFC(0, ca1, Al[0][2], Ah[0][2], "v", Bk[0][5]); GM();
      DSR128(Bk[1][5], Bb, 2560);
      DSR64(Al[1][2], Ab, 2560);
      MFC(0, ca0, Al[0][2], Ah[0][2], "v", Bk[0][2]); GM();
      DSR64(Ah[1][2], Ab, 2568);
      DSR128(Bk[1][2], Bb, 1024);
      MFC(0, ca1, Al[0][3], Ah[0][3], "v", Bk[0][4]); GM();
      DSR64(Al[1][3], Ab, 2048);
      DSR64(Ah[1][3], Ab, 2056);
      WAITL(14);
      MFC(0, ca0, Al[0][3], Ah[0][3], "v", Bk[0][1]); GM();
      DSR128(Bk[1][1], Bb, 512);
      DSR64(Al[1][4], Ab, 1536);
      MFC(0, ca1, Al[0][4], Ah[0][4], "v", Bk[0][3]); GM();
      DSR64(Ah[1][4], Ab, 1544);
      DSR128(Bk[1][0], Bb, 0);
      MFC(0, ca0, Al[0][4], Ah[0][4], "v", Bk[0][0]); GM();
      DSR64(Al[1][5], Ab, 1024);
      DSR64(Ah[1][5], Ab, 1032);
      MFC(0, ca1, Al[0][5], Ah[0][5], "v", Bk[0][2]); GM();
      DSR64(Al[1][6], Ab, 512);
      DSR64(Ah[1][6], Ab, 520);
      MFC(0, ca1, Al[0][6], Ah[0][6], "v", Bk[0][1]); GM();
      DSR64(Al[1][7], Ab, 0);
      DSR64(Ah[1][7], Ab, 8);
      MFC(0, ca1, Al[0][7], Ah[0][7], "v", Bk[0][0]); GM();
      DSR64(ur[0], Ur, 1536);
      DSR64(ur[1], Ur, 0);
      WAITL(14);
      MFC(0, ca1, Al[1][0], Ah[1][0], "v", Bk[1][7]); GM();
      MFC(0, ca0, Al[1][0], Ah[1][0], "v", Bk[1][4]); GM();
      WAITV(0); CHECKF(pollh, v_pc, sCp);
      MFC(0, ca1, Al[1][1], Ah[1][1], "v", Bk[1][6]); GM();
      GLDS1(0, sX1, 0);
      MFC(0, ca0, Al[1][1], Ah[1][1], "v", Bk[1][3]); GM();
      GLDS1(1024, sX1, 1024);
      MFC(0, ca1, Al[1][2], Ah[1][2], "v", Bk[1][5]); GM();
      GLDS1(2048, sX1, 2048);
      MFC(0, ca0, Al[1][2], Ah[1][2], "v", Bk[1][2]); GM();
      GLDS1(3072, sX1, 3072);
      WAITL(12);
      MFC(0, ca1, Al[1][3], Ah[1][3], "v", Bk[1][4]); GM();
      GLDS1(4096, sX1, 4096);
      WAITL(11);
      MFC(0, ca0, Al[1][3], Ah[1][3], "v", Bk[1][1]); GM();
      GLDS1(5120, sX1, 5120);
      WAITL(9);
      MFC(0, ca1, Al[1][4], Ah[1][4], "v", Bk[1][3]); GM();
      GLDS1(6144, sX1, 6144);
      WAITL(8);
      MFC(0, ca0, Al[1][4], Ah[1][4], "v", Bk[1][0]); GM();
      GLDS1(28672, sX1, 229376);
      WAITL(6);
      MFC(0, ca1, Al[1][5], Ah[1][5], "v", Bk[1][2]); GM();
      GLDS1(29696, sX1, 230400);
      WAITL(4);
      MFC(0, ca1, Al[1][6], Ah[1][6], "v", Bk[1][1]); GM();
      GLDS1(30720, sX1, 231424);
      WAITL(2);
      MFC(0, ca1, Al[1][7], Ah[1][7], "v", Bk[1][0]); GM();
      GLDS1(31744, sX1, 232448);
      GLDS1(32768, sX1, 233472);
      GLDS1(33792, sX1, 234496);
      GLDS1(34816, sX1, 235520);
    } else {
      MARK("ROLE w23");
      STUB(6, ".Lwret6"); GM();
      DSR128(Bk[0][6], Bb, 3520);
      DSR64(Al[0][0], Ab, 4032);
      DSR64(Ah[0][0], Ab, 4040);
      DSR128(Bk[0][5], Bb, 3008);
      DSR64(Al[0][1], Ab, 3520);
      DSR64(Ah[0][1], Ab, 3528);
      DSR128(Bk[0][4], Bb, 2496);
      DSR64(Al[0][2], Ab, 3008);
      DSR64(Ah[0][2], Ab, 3016);
      DSR128(Bk[0][3], Bb, 1984);
      DSR64(Al[0][3], Ab, 2496);
      DSR64(Ah[0][3], Ab, 2504);
      WAITL(9);
      MFC(1, ca1, Al[0][0], Ah[0][0], "v", Bk[0][6]); GM();
      DSR128(Bk[0][2], Bb, 1472);
      DSR64(Al[0][4], Ab, 1984);
      WAITL(10);
      MFC(1, ca0, Al[0][0], Ah[0][0], "v", Bk[0][5]); GM();
      DSR64(Ah[0][4], Ab, 1992);
      DSR128(Bk[0][1], Bb, 960);
      WAITL(10);
      MFC(0, ca1, Al[0][1], Ah[0][1], "v", Bk[0][5]); GM();
      DSR64(Al[0][5], Ab, 1472);
      DSR64(Ah[0][5], Ab, 1480);
      WAITL(11);
      MFC(0, ca0, Al[0][1], Ah[0][1], "v", Bk[0][4]); GM();
      DSR128(Bk[0][0], Bb, 448);
      DSR64(Al[0][6], Ab, 960);
      WAITL(11);
      MFC(0, ca1, Al[0][2], Ah[0][2], "v", Bk[0][4]); GM();
      DSR64(Ah[0][6], Ab, 968);
      DSR128(Bk[1][6], Bb, 3456);
      WAITL(12);
      MFC(0, ca0, Al[0][2], Ah[0][2], "v", Bk[0][3]); GM();
      DSR64(Al[1][0], Ab, 3968);
      DSR64(Ah[1][0], Ab, 3976);
      WAITL(12);
      MFC(0, ca1, Al[0][3], Ah[0][3], "v", Bk[0][3]); GM();
      DSR128(Bk[1][5], Bb, 2944);
      DSR64(Al[1][1], Ab, 3456);
      WAITL(13);
      MFC(0, ca0, Al[0][3], Ah[0][3], "v", Bk[0][2]); GM();
      DSR64(Ah[1][1], Ab, 3464);
      DSR128(Bk[1][4], Bb, 2432);
      WAITL(13);
      MFC(0, ca1, Al[0][4], Ah[0][4], "v", Bk[0][2]); GM();
      DSR64(Al[1][2], Ab, 2944);
      DSR64(Ah[1][2], Ab, 2952);
      WAITL(14);
      MFC(0, ca0, Al[0][4], Ah[0][4], "v", Bk[0][1]); GM();
      DSR128(Bk[1][3], Bb, 1920);
      DSR64(Al[1][3], Ab, 2432);
      WAITL(14);
      MFC(0, ca1, Al[0][5], Ah[0][5], "v", Bk[0][1]); GM();
      DSR64(Ah[1][3], Ab, 2440);
      DSR128(Bk[1][2], Bb, 1408);
      WAITL(14);
      MFC(0, ca0, Al[0][5], Ah[0][5], "v", Bk[0][0]); GM();
      DSR64(Al[1][4], Ab, 1920);
      DSR64(Ah[1][4], Ab, 1928);
      WAITL(14);
      MFC(0, ca1, Al[0][6], Ah[0][6], "v", Bk[0][0]); GM();
      DSR128(Bk[1][1], Bb, 896);
      DSR64(Al[1][5], Ab, 1408);
      WAITL(14);
      MFC(0, ca1, Al[1][0], Ah[1][0], "v", Bk[1][6]); GM();
      DSR64(Ah[1][5], Ab, 1416);
      DSR128(Bk[1][0], Bb, 384);
      WAITL(14);
      MFC(0, ca0, Al[1][0], Ah[1][0], "v", Bk[1][5]); STUB(7, ".Lwret7"); GM();
      DSR64(Al[1][6], Ab, 896);
      DSR64(Ah[1][6], Ab, 904);
      WAITL(14);
      MFC(0, ca1, Al[1][1], Ah[1][1], "v", Bk[1][5]); GM();
      DSR128(Bk[0][6], Bb, 3392);
      DSR64(Al[0][0], Ab, 3904);
      MFC(0, ca0, Al[1][1], Ah[1][1], "v", Bk[1][4]); GM();
      DSR64(Ah[0][0], Ab, 3912);
      DSR128(Bk[0][5], Bb, 2880);
      WAITL(14);
      MFC(0, ca1, Al[1][2], Ah[1][2], "v", Bk[1][4]); GM();
      DSR64(Al[0][1], Ab, 3392);
      DSR64(Ah[0][1], Ab, 3400);
      MFC(0, ca0, Al[1][2], Ah[1][2], "v", Bk[1][3]); GM();
      DSR128(Bk[0][4], Bb, 2368);
      DSR64(Al[0][2], Ab, 2880);
      WAITL(14);
      MFC(0, ca1, Al[1][3], Ah[1][3], "v", Bk[1][3]); GM();
      DSR64(Ah[0][2], Ab, 2888);
      DSR128(Bk[0][3], Bb, 1856);
      MFC(0, ca0, Al[1][3], Ah[1][3], "v", Bk[1][2]); GM();
      DSR64(Al[0][3], Ab, 2368);
      DSR64(Ah[0][3], Ab, 2376);
      MFC(0, ca1, Al[1][4], Ah[1][4], "v", Bk[1][2]); GM();
      DSR128(Bk[0][2], Bb, 1344);
      DSR64(Al[0][4], Ab, 1856);
      WAITL(14);
      MFC(0, ca0, Al[1][4], Ah[1][4], "v", Bk[1][1]); GM();
      DSR64(Ah[0][4], Ab, 1864);
      DSR128(Bk[0][1], Bb, 832);
      WAITV(6); FLAG0();
      MFC(0, ca1, Al[1][5], Ah[1][5], "v", Bk[1][1]); GM();
      DSR64(Al[0][5], Ab, 1344);
      DSR64(Ah[0][5], Ab, 1352);
      BLOADWD(wd[0][0], 0);
      MFC(0, ca0, Al[1][5], Ah[1][5], "v", Bk[1][0]); GM();
      DSR128(Bk[0][0], Bb, 320);
      DSR64(Al[0][6], Ab, 832);
      MFC(0, ca1, Al[1][6], Ah[1][6], "v", Bk[1][0]); GM();
      DSR64(Ah[0][6], Ab, 840);
      BLOADWD(wd[0][1], 57344);
      WAITL(14);
      MFC(0, ca1, Al[0][0], Ah[0][0], "v", Bk[0][6]); GM();
      DSR128(Bk[1][6], Bb, 3328);
      DSR64(Al[1][0], Ab, 3840);
      MFC(0, ca0, Al[0][0], Ah[0][0], "v", Bk[0][5]); GM();
      DSR64(Ah[1][0], Ab, 3848);
      DSR128(Bk[1][5], Bb, 2816);
      BLOADWD(wd[0][2], 114688);
      MFC(0, ca1, Al[0][1], Ah[0][1], "v", Bk[0][5]); GM();
      DSR64(Al[1][1], Ab, 3328);
      DSR64(Ah[1][1], Ab, 3336);
      MFC(0, ca0, Al[0][1], Ah[0][1], "v", Bk[0][4]); GM();
      DSR128(Bk[1][4], Bb, 2304);
      DSR64(Al[1][2], Ab, 2816);
      BLOADWD(wd[1][0], 1024);
      WAITL(14);
      MFC(0, ca1, Al[0][2], Ah[0][2], "v", Bk[0][4]); GM();
      DSR64(Ah[1][2], Ab, 2824);
      DSR128(Bk[1][3], Bb, 1792);
      MFC(0, ca0, Al[0][2], Ah[0][2], "v", Bk[0][3]); GM();
      DSR64(Al[1][3], Ab, 2304);
      DSR64(Ah[1][3], Ab, 2312);
      BLOADWD(wd[1][1], 58368);
      MFC(0, ca1, Al[0][3], Ah[0][3], "v", Bk[0][3]); GM();
      DSR128(Bk[1][2], Bb, 1280);
      DSR64(Al[1][4], Ab, 1792);
      MFC(0, ca0, Al[0][3], Ah[0][3], "v", Bk[0][2]); GM();
      DSR64(Ah[1][4], Ab, 1800);
      DSR128(Bk[1][1], Bb, 768);
      BLOADWD(wd[1][2], 115712);
      MFC(0, ca1, Al[0][4], Ah[0][4], "v", Bk[0][2]); GM();
      DSR64(Al[1][5], Ab, 1280);
      DSR64(Ah[1][5], Ab, 1288);
      WAITL(14);
      MFC(0, ca0, Al[0][4], Ah[0][4], "v", Bk[0][1]); GM();
      DSR128(Bk[1][0], Bb, 256);
      DSR64(Al[1][6], Ab, 768);
      BLOADWD(wd[2][0], 2048);
      MFC(0, ca1, Al[0][5], Ah[0][5], "v", Bk[0][1]); GM();
      DSR64(Ah[1][6], Ab, 776);
      MFC(0, ca0, Al[0][5], Ah[0][5], "v", Bk[0][0]); GM();
      BLOADWD(wd[2][1], 59392);
      MFC(0, ca1, Al[0][6], Ah[0][6], "v", Bk[0][0]); GM();
      MFC(0, ca1, Al[1][0], Ah[1][0], "v", Bk[1][6]); GM();
      DSR128(Bk[0][6], Bb, 3264);
      DSR64(Al[0][0], Ab, 3776);
      BLOADWD(wd[2][2], 116736);
      MFC(0, ca0, Al[1][0], Ah[1][0], "v", Bk[1][5]); GM();
      DSR64(Ah[0][0], Ab, 3784);
      DSR128(Bk[0][5], Bb, 2752);
      WAITL(14);
      MFC(0, ca1, Al[1][1], Ah[1][1], "v", Bk[1][5]); GM();
      DSR64(Al[0][1], Ab, 3264);
      DSR64(Ah[0][1], Ab, 3272);
      BLOADWD(wd[3][0], 3072);
      MFC(0, ca0, Al[1][1], Ah[1][1], "v", Bk[1][4]); GM();
      DSR128(Bk[0][4], Bb, 2240);
      DSR64(Al[0][2], Ab, 2752);
      MFC(0, ca1, Al[1][2], Ah[1][2], "v", Bk[1][4]); GM();
      DSR64(Ah[0][2], Ab, 2760);
      DSR128(Bk[0][3], Bb, 1728);
      BLOADWD(wd[3][1], 60416);
      MFC(0, ca0, Al[1][2], Ah[1][2], "v", Bk[1][3]); GM();
      DSR64(Al[0][3], Ab, 2240);
      DSR64(Ah[0][3], Ab, 2248);
      WAITL(14);
      MFC(0, ca1, Al[1][3], Ah[1][3], "v", Bk[1][3]); GM();
      DSR128(Bk[0][2], Bb, 1216);
      DSR64(Al[0][4], Ab, 1728);
      BLOADWD(wd[3][2], 117760);
      MFC(0, ca0, Al[1][3], Ah[1][3], "v", Bk[1][2]); GM();
      DSR64(Ah[0][4], Ab, 1736);
      DSR128(Bk[0][1], Bb, 704);
      MFC(0, ca1, Al[1][4], Ah[1][4], "v", Bk[1][2]); GM();
      DSR64(Al[0][5], Ab, 1216);
      DSR64(Ah[0][5], Ab, 1224);
      BLOADWD(wd[4][0], 4096);
      MFC(0, ca0, Al[1][4], Ah[1][4], "v", Bk[1][1]); GM();
      DSR128(Bk[0][0], Bb, 192);
      DSR64(Al[0][6], Ab, 704);
      MFC(0, ca1, Al[1][5], Ah[1][5], "v", Bk[1][1]); GM();
      DSR64(Ah[0][6], Ab, 712);
      BLOADWD(wd[4][1], 61440);
      MFC(0, ca0, Al[1][5], Ah[1][5], "v", Bk[1][0]); GM();
      WAITL(14);
      MFC(0, ca1, Al[1][6], Ah[1][6], "v", Bk[1][0]); GM();
      BLOADWD(wd[4][2], 118784);
      MFC(0, ca1, Al[0][0], Ah[0][0], "v", Bk[0][6]); GM();
      DSR128(Bk[1][6], Bb, 3200);
      DSR64(Al[1][0], Ab, 3712);
      MFC(0, ca0, Al[0][0], Ah[0][0], "v", Bk[0][5]); GM();
      DSR64(Ah[1][0], Ab, 3720);
      DSR128(Bk[1][5], Bb, 2688);
      BLOADWD(wd[5][0], 5120);
      MFC(0, ca1, Al[0][1], Ah[0][1], "v", Bk[0][5]); GM();
      DSR64(Al[1][1], Ab, 3200);
      DSR64(Ah[1][1], Ab, 3208);
      MFC(0, ca0, Al[0][1], Ah[0][1], "v", Bk[0][4]); GM();
      DSR128(Bk[1][4], Bb, 2176);
      DSR64(Al[1][2], Ab, 2688);
      BLOADWD(wd[5][1], 62464);
      WAITL(14);
      MFC(0, ca1, Al[0][2], Ah[0][2], "v", Bk[0][4]); GM();
      DSR64(Ah[1][2], Ab, 2696);
      DSR128(Bk[1][3], Bb, 1664);
      MFC(0, ca0, Al[0][2], Ah[0][2], "v", Bk[0][3]); GM();
      DSR64(Al[1][3], Ab, 2176);
      DSR64(Ah[1][3], Ab, 2184);
      BLOADWD(wd[5][2], 119808);
      MFC(0, ca1, Al[0][3], Ah[0][3], "v", Bk[0][3]); GM();
      DSR128(Bk[1][2], Bb, 1152);
      DSR64(Al[1][4], Ab, 1664);
      MFC(0, ca0, Al[0][3], Ah[0][3], "v", Bk[0][2]); GM();
      DSR64(Ah[1][4], Ab, 1672);
      DSR128(Bk[1][1], Bb, 640);
      BLOADWD(wd[6][0], 6144);
      MFC(0, ca1, Al[0][4], Ah[0][4], "v", Bk[0][2]); GM();
      DSR64(Al[1][5], Ab, 1152);
      DSR64(Ah[1][5], Ab, 1160);
      WAITL(14);
      MFC(0, ca0, Al[0][4], Ah[0][4], "v", Bk[0][1]); GM();
      DSR128(Bk[1][0], Bb, 128);
      DSR64(Al[1][6], Ab, 640);
      BLOADWD(wd[6][1], 63488);
      MFC(0, ca1, Al[0][5], Ah[0][5], "v", Bk[0][1]); GM();
      DSR64(Ah[1][6], Ab, 648);
      MFC(0, ca0, Al[0][5], Ah[0][5], "v", Bk[0][0]); GM();
      BLOADWD(wd[6][2], 120832);
      MFC(0, ca1, Al[0][6], Ah[0][6], "v", Bk[0][0]); GM();
      MFC(0, ca1, Al[1][0], Ah[1][0], "v", Bk[1][6]); GM();
      DSR128(Bk[0][6], Bb, 3136);
      DSR64(Al[0][0], Ab, 3648);
      BLOADWD(wd[7][0], 7168);
      MFC(0, ca0, Al[1][0], Ah[1][0], "v", Bk[1][5]); GM();
      DSR64(Ah[0][0], Ab, 3656);
      DSR128(Bk[0][5], Bb, 2624);
      WAITL(14);
      MFC(0, ca1, Al[1][1], Ah[1][1], "v", Bk[1][5]); GM();
      DSR64(Al[0][1], Ab, 3136);
      DSR64(Ah[0][1], Ab, 3144);
      BLOADWD(wd[7][1], 64512);
      MFC(0, ca0, Al[1][1], Ah[1][1], "v", Bk[1][4]); GM();
      DSR128(Bk[0][4], Bb, 2112);
      DSR64(Al[0][2], Ab, 2624);
      MFC(0, ca1, Al[1][2], Ah[1][2], "v", Bk[1][4]); GM();
      DSR64(Ah[0][2], Ab, 2632);
      DSR128(Bk[0][3], Bb, 1600);
      BLOADWD(wd[7][2], 121856);
      MFC(0, ca0, Al[1][2], Ah[1][2], "v", Bk[1][3]); GM();
      DSR64(Al[0][3], Ab, 2112);
      DSR64(Ah[0][3], Ab, 2120);
      WAITL(14);
      MFC(0, ca1, Al[1][3], Ah[1][3], "v", Bk[1][3]); GM();
      DSR128(Bk[0][2], Bb, 1088);
      DSR64(Al[0][4], Ab, 1600);
      MFC(0, ca0, Al[1][3], Ah[1][3], "v", Bk[1][2]); GM();
      DSR64(Ah[0][4], Ab, 1608);
      DSR128(Bk[0][1], Bb, 576);
      MFC(0, ca1, Al[1][4], Ah[1][4], "v", Bk[1][2]); GM();
      DSR64(Al[0][5], Ab, 1088);
      DSR64(Ah[0][5], Ab, 1096);
      BLOAD2(pollh, v_pc, sCp);
      MFC(0, ca0, Al[1][4], Ah[1][4], "v", Bk[1][1]); GM();
      DSR128(Bk[0][0], Bb, 64);
      DSR64(Al[0][6], Ab, 576);
      MFC(0, ca1, Al[1][5], Ah[1][5], "v", Bk[1][1]); GM();
      DSR64(Ah[0][6], Ab, 584);
      MFC(0, ca0, Al[1][5], Ah[1][5], "v", Bk[1][0]); GM();
      WAITL(14);
      MFC(0, ca1, Al[1][6], Ah[1][6], "v", Bk[1][0]); GM();
      MFC(0, ca1, Al[0][0], Ah[0][0], "v", Bk[0][6]); GM();
      DSR128(Bk[1][6], Bb, 3072);
      DSR64(Al[1][0], Ab, 3584);
      MFC(0, ca0, Al[0][0], Ah[0][0], "v", Bk[0][5]); GM();
      DSR64(Ah[1][0], Ab, 3592);
      DSR128(Bk[1][5], Bb, 2560);
      MFC(0, ca1, Al[0][1], Ah[0][1], "v", Bk[0][5]); GM();
      DSR64(Al[1][1], Ab, 3072);
      DSR64(Ah[1][1], Ab, 3080);
      MFC(0, ca0, Al[0][1], Ah[0][1], "v", Bk[0][4]); GM();
      DSR128(Bk[1][4], Bb, 2048);
      DSR64(Al[1][2], Ab, 2560);
      WAITL(14);
      MFC(0, ca1, Al[0][2], Ah[0][2], "v", Bk[0][4]); GM();
      DSR64(Ah[1][2], Ab, 2568);
      DSR128(Bk[1][3], Bb, 1536);
      MFC(0, ca0, Al[0][2], Ah[0][2], "v", Bk[0][3]); GM();
      DSR64(Al[1][3], Ab, 2048);
      DSR64(Ah[1][3], Ab, 2056);
      MFC(0, ca1, Al[0][3], Ah[0][3], "v", Bk[0][3]); GM();
      DSR128(Bk[1][2], Bb, 1024);
      DSR64(Al[1][4], Ab, 1536);
      MFC(0, ca0, Al[0][3], Ah[0][3], "v", Bk[0][2]); GM();
      DSR64(Ah[1][4], Ab, 1544);
      DSR128(Bk[1][1], Bb, 512);
      MFC(0, ca1, Al[0][4], Ah[0][4], "v", Bk[0][2]); GM();
      DSR64(Al[1][5], Ab, 1024);
      DSR64(Ah[1][5], Ab, 1032);
      WAITL(14);
      MFC(0, ca0, Al[0][4], Ah[0][4], "v", Bk[0][1]); GM();
      DSR128(Bk[1][0], Bb, 0);
      DSR64(Al[1][6], Ab, 512);
      MFC(0, ca1, Al[0][5], Ah[0][5], "v", Bk[0][1]); GM();
      DSR64(Ah[1][6], Ab, 520);
      DSR64(ur[0], Ur, 1024);
      MFC(0, ca0, Al[0][5], Ah[0][5], "v", Bk[0][0]); GM();
      DSR64(ur[1], Ur, 512);
      MFC(0, ca1, Al[0][6], Ah[0][6], "v", Bk[0][0]); GM();
      MFC(0, ca1, Al[1][0], Ah[1][0], "v", Bk[1][6]); GM();
      MFC(0, ca0, Al[1][0], Ah[1][0], "v", Bk[1][5]); GM();
      WAITV(0); CHECKF(pollh, v_pc, sCp);
      WAITL(14);
      MFC(0, ca1, Al[1][1], Ah[1][1], "v", Bk[1][5]); GM();
      GLDS1(0, sX1, 0);
      MFC(0, ca0, Al[1][1], Ah[1][1], "v", Bk[1][4]); GM();
      GLDS1(1024, sX1, 1024);
      MFC(0, ca1, Al[1][2], Ah[1][2], "v", Bk[1][4]); GM();
      GLDS1(2048, sX1, 2048);
      WAITL(13);
      MFC(0, ca0, Al[1][2], Ah[1][2], "v", Bk[1][3]); GM();
      GLDS1(3072, sX1, 3072);
      WAITL(11);
      MFC(0, ca1, Al[1][3], Ah[1][3], "v", Bk[1][3]); GM();
      GLDS1(4096, sX1, 4096);
      WAITL(10);
      MFC(0, ca0, Al[1][3], Ah[1][3], "v", Bk[1][2]); GM();
      GLDS1(5120, sX1, 5120);
      WAITL(8);
      MFC(0, ca1, Al[1][4], Ah[1][4], "v", Bk[1][2]); GM();
      GLDS1(6144, sX1, 6144);
      WAITL(7);
      MFC(0, ca0, Al[1][4], Ah[1][4], "v", Bk[1][1]); GM();
      GLDS1(28672, sX1, 229376);
      WAITL(5);
      MFC(0, ca1, Al[1][5], Ah[1][5], "v", Bk[1][1]); GM();
      GLDS1(29696, sX1, 230400);
      WAITL(4);
      MFC(0, ca0, Al[1][5], Ah[1][5], "v", Bk[1][0]); GM();
      GLDS1(30720, sX1, 231424);
      WAITL(2);
      MFC(0, ca1, Al[1][6], Ah[1][6], "v", Bk[1][0]); GM();
      GLDS1(31744, sX1, 232448);
      GLDS1(32768, sX1, 233472);
      GLDS1(33792, sX1, 234496);
      GLDS1(34816, sX1, 235520);
    }
    WAITL(0); NOPS(); SB(); STUB(8, ".Lwret8"); GM();
    CONV_EPI(4 + bh, 7 - bh, v_cv, ca0, ca1, ur[0], ur[1]); GM();

  }
  const unsigned sCf2 = w == 0 ? OFF_CF + (224 + b) * 128 : 0xF0000000u;
  v4u bext = {0u, 0u, 0u, 0u};
  float vinvD = 1.0f / D;
  const float sEps = 1e-5f;
  v2u gp = {0u, e1};
  unsigned sXg[4];
#pragma unroll
  for (int g = 0; g < 4; ++g) sXg[g] = ((g + w) & 3) * 1024;
  v4u H[14], xf[8], gA, gB, gC, gD, hfr;
  v2u xo;
  f32x16 acc[2];
  f32x4 acc2[2][4], sacc, stv, pr[4];
  TIE(sel); TIE(aext); TIE(bext); TIE(vinvD); TIE(gp);
  float er0;
  float er1;
  float er2;
  float er3;
  float ey0;
  float ey1;
  float ey2;
  float ey3;
  unsigned xadr[2];
  float mt0;
  float mt1;
  unsigned mtp;
  float ep0;
  float ep1;
  unsigned hp0;
  unsigned hp1;
  unsigned hp2;
  unsigned hp3;
  unsigned rp0_0;
  unsigned rp0_1;
  unsigned yp0_0;
  unsigned yp0_1;
  float ea;
  float eb;
  float ec;
  float et;
  float eu;
  unsigned rp1_0;
  unsigned rp1_1;
  unsigned yp1_0;
  unsigned yp1_1;
  unsigned rp2_0;
  unsigned rp2_1;
  unsigned yp2_0;
  unsigned yp2_1;
  float rf0;
  float rf1;
  float rf2;
  float rf3;
  float yf0_0;
  float yf0_1;
  float yf0_2;
  float yf0_3;
  unsigned rw1;
  unsigned rw2;
  unsigned rw3;
  float cp0;
  float cq0;
  float cm0;
  float cv0;
  float sy0;
  unsigned ct0;
  unsigned rp3_0;
  unsigned rp3_1;
  float rsq0;
  unsigned yp3_0;
  unsigned yp3_1;
  float fo0;
  float fo1;
  float fo2;
  float fo3;
  unsigned fp0;
  unsigned fp1;
  float x20_0;
  float x20_1;
  float x20_2;
  float x20_3;
  float fa;
  float fb;
  float fc;
  float ft0;
  float fu;
  float cp1;
  float cq1;
  float cm1;
  float cv1;
  float sy1;
  unsigned ct1;
  float rsq1;
  float yf1_0;
  float yf1_1;
  float yf1_2;
  float yf1_3;
  float sy2;
  float rsq2;
  float x21_0;
  float x21_1;
  float x21_2;
  float x21_3;
  float xp;
  float xq;
  float xm;
  float xv;
  float xy;
  unsigned xc;
  float xmb;
  float xt0;
  float xt1;
  float xt2;
  float xt3;
  unsigned x3p0_0;
  unsigned x3p0_1;
  unsigned xo0;
  unsigned xo1;
  float xd0;
  float dm0;
  float sy3;
  float rsq3;
  unsigned x3p1_0;
  unsigned x3p1_1;
  float xd1;
  float dm1;
  unsigned x3p2_0;
  unsigned x3p2_1;
  float xd2;
  float dm2;
  float kp;
  float kv;
  unsigned kt;
  float rsq4;
  unsigned x3p3_0;
  unsigned x3p3_1;
  float xd3;
  float dm3;
  float rsq5;
  float rsq6;
  float rsq7;

  BLOADWD(wd[8][0], 8192);
  BLOADWD(wd[8][1], 65536);
  SB(); GM();
  BLOADWD(wd[8][2], 122880);
  SB(); GM();
  BLOADWD(wd[9][0], 9216);
  SB(); GM();
  BLOADWD(wd[9][1], 66560);
  SB(); GM();
  BLOADWD(wd[9][2], 123904);
  SB(); GM();
  WAITV(15);
  SBAR();
  asm volatile("v_add_u32 %0, %1, %2" : "=v"(xadr[0]) : "s"(sXg[0] + 0u), "v"(xl0));
  DSRTR(xf[0], xadr[0], 0, 256);
  DSRTR(xf[1], xadr[0], 4096, 4352);
  DSRTR(xf[2], xadr[0], 8192, 8448);
  DSRTR(xf[3], xadr[0], 12288, 12544);
  DSRTR(xf[4], xadr[0], 16384, 16640);
  WAITL(8);
  MF32Z("v", acc[0], wg[0], xf[0]);
  DSRTR(xf[5], xadr[0], 20480, 20736);
  asm volatile("v_add_u32 %0, %1, %2" : "=v"(xadr[1]) : "s"(sXg[1] + 0u), "v"(xl0));
  GLDSR(0, 0);
  MF16Z("v", sacc, sel, xf[0]);
  SB(); GM();
  WAITL(8);
  MF32("v", acc[0], wg[1], xf[1]);
  DSRTR(xf[6], xadr[0], 24576, 24832);
  GLDSR(4096, 458752);
  MF16("v", sacc, sel, xf[1]);
  SB(); GM();
  WAITL(8);
  MF32("v", acc[0], wg[2], xf[2]);
  DSRTR(xf[7], xadr[1], 0, 256);
  MF16("v", sacc, sel, xf[2]);
  SB(); GM();
  WAITL(8);
  MF32("v", acc[0], wg[3], xf[3]);
  DSRTR(xf[0], xadr[1], 4096, 4352);
  MF16("v", sacc, sel, xf[3]);
  SB(); GM();
  WAITL(8);
  MF32("v", acc[0], wg[4], xf[4]);
  DSRTR(xf[1], xadr[1], 8192, 8448);
  GLDS1(114688, sX1, 458752);
  MF16("v", sacc, sel, xf[4]);
  SB(); GM();
  WAITL(8);
  MF32("v", acc[0], wg[5], xf[5]);
  DSRTR(xf[2], xadr[1], 12288, 12544);
  MF16("v", sacc, sel, xf[5]);
  GLDS1(115712, sX1, 459776);
  SB(); GM();
  WAITL(8);
  MF32("a", acc[0], wg[6], xf[6]);
  DSRTR(xf[3], xadr[1], 16384, 16640);
  MF16("v", sacc, sel, xf[6]);
  GLDS1(116736, sX1, 460800);
  SB(); GM();
  WAITL(8);
  MF32("a", acc[0], wg[7], xf[7]);
  DSRTR(xf[4], xadr[1], 20480, 20736);
  asm volatile("v_add_u32 %0, %1, %2" : "=v"(xadr[0]) : "s"(sXg[2] + 0u), "v"(xl0));
  SB(); GM();
  WAITL(8);
  MF32("a", acc[0], wg[8], xf[0]);
  DSRTR(xf[5], xadr[1], 24576, 24832);
  WAITV(11);
  SBAR();
  BSTORE2(tagv, v_one, sCf2);
  SB(); GM();
  WAITL(8);
  MF32("a", acc[0], wg[9], xf[1]);
  DSRTR(xf[6], xadr[0], 0, 256);
  GLDS1(117760, sX1, 461824);
  SB(); GM();
  WAITL(8);
  MF32("a", acc[0], wg[10], xf[2]);
  DSRTR(xf[7], xadr[0], 4096, 4352);
  { asm volatile("" :: "v"(sacc)); float s0_ = sacc[0]; DSW32(st_wr, s0_, 0); }
  SB(); GM();
  WAITL(9);
  MF32("a", acc[0], wg[11], xf[3]);
  DSRTR(xf[0], xadr[0], 8192, 8448);
  GLDS1(118784, sX1, 462848);
  SB(); GM();
  WAITL(9);
  MF32("a", acc[0], wg[12], xf[4]);
  DSRTR(xf[1], xadr[0], 12288, 12544);
  SB(); GM();
  WAITL(9);
  MF32("a", acc[0], wg[13], xf[5]);
  DSRTR(xf[2], xadr[0], 16384, 16640);
  GLDS1(119808, sX1, 463872);
  SB(); GM();
  WAITL(9);
  MF32("a", acc[0], wg[14], xf[6]);
  DSRTR(xf[3], xadr[0], 20480, 20736);
  asm volatile("v_add_u32 %0, %1, %2" : "=v"(xadr[1]) : "s"(sXg[3] + 0u), "v"(xl0));
  SB(); STUB(9, ".Lwret9"); GM();
  WAITL(9);
  MF32("a", acc[0], wg[15], xf[7]);
  DSRTR(xf[4], xadr[0], 24576, 24832);
  GLDS1(120832, sX1, 464896);
  SB(); GM();
  WAITL(8);
  MF32("a", acc[0], wg[16], xf[0]);
  DSRTR(xf[5], xadr[1], 0, 256);
  SBAR();
  DSR128(stv, st_rd, 0);
  SB(); GM();
  WAITL(9);
  MF32("a", acc[0], wg[17], xf[1]);
  DSRTR(xf[6], xadr[1], 4096, 4352);
  BLOADWD(wd[10][0], 10240);
  SB(); GM();
  WAITL(9);
  MF32("a", acc[0], wg[18], xf[2]);
  DSRTR(xf[7], xadr[1], 8192, 8448);
  SB(); GM();
  WAITL(9);
  MF32("a", acc[0], wg[19], xf[3]);
  DSRTR(xf[0], xadr[1], 12288, 12544);
  BLOADWD(wd[10][1], 67584);
  WAITL(6);
  SB(); GM();
  MF32("a", acc[0], wg[20], xf[4]);
  DSRTR(xf[1], xadr[1], 16384, 16640);
  asm volatile("v_add_f32 %0, %2, %3\n\tv_add_f32 %1, %4, %5\n\tv_add_f32 %0, %0, %1" : "=&v"(mt0), "=&v"(mt1) : "v"(stv[0]), "v"(stv[1]), "v"(stv[2]), "v"(stv[3]));
  SB(); GM();
  MF32("a", acc[0], wg[21], xf[5]);
  DSRTR(xf[2], xadr[1], 20480, 20736);
  asm volatile("v_add_u32 %0, %1, %2" : "=v"(xadr[0]) : "s"(sXg[0] + 28672u), "v"(xl0));
  BLOADWD(wd[10][2], 124928);
  SB(); GM();
  WAITL(8);
  MF32("a", acc[0], wg[22], xf[6]);
  DSRTR(xf[3], xadr[1], 24576, 24832);
  asm volatile("v_mul_f32 %0, 0xbb124925, %0\n\tv_cvt_pk_bf16_f32 %1, %0, %0" : "+v"(mt0), "=&v"(mtp) : );
  bext[0] = mtp;
  SB(); GM();
  WAITL(8);
  MF32("a", acc[0], wg[23], xf[7]);
  SBAR();
  DSRTR(xf[4], xadr[0], 0, 256);
  BLOADWD(wd[11][0], 11264);
  SB(); GM();
  WAITL(8);
  MF32("a", acc[0], wg[24], xf[0]);
  DSRTR(xf[5], xadr[0], 4096, 4352);
  SB(); GM();
  WAITL(8);
  MF32("a", acc[0], wg[25], xf[1]);
  DSRTR(xf[6], xadr[0], 8192, 8448);
  BLOADWD(wd[11][1], 68608);
  SB(); GM();
  WAITL(8);
  MF32("a", acc[0], wg[26], xf[2]);
  DSRTR(xf[7], xadr[0], 12288, 12544);
  SB(); GM();
  WAITL(8);
  MF32("a", acc[0], wg[27], xf[3]);
  DSRTR(xf[0], xadr[0], 16384, 16640);
  BLOADWD(wd[11][2], 125952);
  SB(); GM();
  MF32("v", acc[0], aext, bext);
  DSRTR(xf[1], xadr[0], 20480, 20736);
  asm volatile("v_add_u32 %0, %1, %2" : "=v"(xadr[1]) : "s"(sXg[1] + 28672u), "v"(xl0));
  SB(); GM();
  WAITL(10);
  MF32Z("v", acc[1], wg[0], xf[4]);
  BLOADWD(wd[12][0], 12288);
  MF16Z("v", sacc, sel, xf[4]);
  SBAR();
  SB(); GM();
  WAITL(8);
  MF32("v", acc[1], wg[1], xf[5]);
  DSRTR(xf[2], xadr[0], 24576, 24832);
  MF16("v", sacc, sel, xf[5]);
  GLDS1(0, sX1, 688128);
  SB(); GM();
  WAITL(8);
  MF32("v", acc[1], wg[2], xf[6]);
  DSRTR(xf[3], xadr[1], 0, 256);
  asm volatile("v_max_f32 %0, 0, %2\n\tv_max_f32 %1, 0, %3\n\tv_mul_f32 %0, %0, %4" : "=&v"(ep0), "=&v"(ep1) : "v"(acc[0][0]), "v"(acc[0][1]), "v"(acc[0][8]));
  MF16("v", sacc, sel, xf[6]);
  GLDS1(1024, sX1, 689152);
  SB(); GM();
  WAITL(8);
  MF32("v", acc[1], wg[3], xf[7]);
  DSRTR(xf[4], xadr[1], 4096, 4352);
  asm volatile("v_mul_f32 %0, %0, %3\n\tv_cvt_pk_bf16_f32 %1, %2, %0\n\tv_max_f32 %2, 0, %4" : "+v"(ep1), "=&v"(hp0), "+v"(ep0) : "v"(acc[0][9]), "v"(acc[0][2]));
  MF16("v", sacc, sel, xf[7]);
  asm volatile("v_max_f32 %0, 0, %2\n\tv_mul_f32 %1, %1, %3" : "=&v"(ep1), "+v"(ep0) : "v"(acc[0][3]), "v"(acc[0][10]));
  SB(); GM();
  WAITL(8);
  MF32("v", acc[1], wg[4], xf[0]);
  DSRTR(xf[5], xadr[1], 8192, 8448);
  GLDS1(2048, sX1, 690176);
  MF16("v", sacc, sel, xf[0]);
  asm volatile("v_mul_f32 %0, %0, %2\n\tv_cvt_pk_bf16_f32 %1, %3, %0" : "+v"(ep1), "=&v"(hp1) : "v"(acc[0][11]), "v"(ep0));
  SB(); GM();
  WAITL(8);
  MF32("v", acc[1], wg[5], xf[1]);
  DSRTR(xf[6], xadr[1], 12288, 12544);
  asm volatile("v_max_f32 %0, 0, %2\n\tv_max_f32 %1, 0, %3\n\tv_mul_f32 %0, %0, %4" : "=&v"(ep0), "=&v"(ep1) : "v"(acc[0][4]), "v"(acc[0][5]), "v"(acc[0][12]));
  MF16("v", sacc, sel, xf[1]);
  GLDS1(3072, sX1, 691200);
  SB(); GM();
  WAITL(8);
  MF32("a", acc[1], wg[6], xf[2]);
  DSRTR(xf[7], xadr[1], 16384, 16640);
  asm volatile("v_mul_f32 %0, %0, %3\n\tv_cvt_pk_bf16_f32 %1, %2, %0\n\tv_max_f32 %2, 0, %4" : "+v"(ep1), "=&v"(hp2), "+v"(ep0) : "v"(acc[0][13]), "v"(acc[0][6]));
  MF16("v", sacc, sel, xf[2]);
  GLDS1(4096, sX1, 692224);
  SB(); GM();
  WAITL(8);
  MF32("a", acc[1], wg[7], xf[3]);
  DSRTR(xf[0], xadr[1], 20480, 20736);
  asm volatile("v_add_u32 %0, %1, %2" : "=v"(xadr[0]) : "s"(sXg[2] + 28672u), "v"(xl0));
  asm volatile("v_max_f32 %0, 0, %2\n\tv_mul_f32 %1, %1, %3" : "=&v"(ep1), "+v"(ep0) : "v"(acc[0][7]), "v"(acc[0][14]));
  SB(); GM();
  WAITL(8);
  MF32("a", acc[1], wg[8], xf[4]);
  DSRTR(xf[1], xadr[1], 24576, 24832);
  GLDS1(5120, sX1, 693248);
  SB(); GM();
  WAITL(8);
  MF32("a", acc[1], wg[9], xf[5]);
  DSRTR(xf[2], xadr[0], 0, 256);
  { asm volatile("" :: "v"(sacc)); float s0_ = sacc[0]; DSW32(st_wr, s0_, 512); }
  asm volatile("v_mul_f32 %0, %0, %2\n\tv_cvt_pk_bf16_f32 %1, %3, %0" : "+v"(ep1), "=&v"(hp3) : "v"(acc[0][15]), "v"(ep0));
  SB(); GM();
  WAITL(9);
  MF32("a", acc[1], wg[10], xf[6]);
  DSRTR(xf[3], xadr[0], 4096, 4352);
  GLDS1(6144, sX1, 694272);
  SB(); GM();
  WAITL(9);
  MF32("a", acc[1], wg[11], xf[7]);
  DSRTR(xf[4], xadr[0], 8192, 8448);
  { hfr = v4u{hp0, hp1, hp2, hp3}; const unsigned so_ = sHst + 0 * H_LAYER + 0u; BSTORE4(hfr, v_hst, so_); }
  SB(); GM();
  WAITL(9);
  MF32("a", acc[1], wg[12], xf[0]);
  DSRTR(xf[5], xadr[0], 12288, 12544);
  SB(); GM();
  WAITL(9);
  MF32("a", acc[1], wg[13], xf[1]);
  DSRTR(xf[6], xadr[0], 16384, 16640);
  SB(); GM();
  WAITL(9);
  MF32("a", acc[1], wg[14], xf[2]);
  DSRTR(xf[7], xadr[0], 20480, 20736);
  asm volatile("v_add_u32 %0, %1, %2" : "=v"(xadr[1]) : "s"(sXg[3] + 28672u), "v"(xl0));
  SB(); GM();
  WAITL(8);
  MF32("a", acc[1], wg[15], xf[3]);
  DSRTR(xf[0], xadr[0], 24576, 24832);
  SBAR();
  DSR128(stv, st_rd, 512);
  SB(); GM();
  WAITL(9);
  MF32("a", acc[1], wg[16], xf[4]);
  DSRTR(xf[1], xadr[1], 0, 256);
  WAITV(23);
  SB(); GM();
  WAITL(9);
  MF32("a", acc[1], wg[17], xf[5]);
  DSRTR(xf[2], xadr[1], 4096, 4352);
  SB(); GM();
  WAITL(9);
  MF32("a", acc[1], wg[18], xf[6]);
  DSRTR(xf[3], xadr[1], 8192, 8448);
  WAITL(6);
  asm volatile("v_add_f32 %0, %2, %3\n\tv_add_f32 %1, %4, %5" : "=&v"(mt0), "=&v"(mt1) : "v"(stv[0]), "v"(stv[1]), "v"(stv[2]), "v"(stv[3]));
  SB(); GM();
  MF32("a", acc[1], wg[19], xf[7]);
  DSRTR(xf[4], xadr[1], 12288, 12544);
  asm volatile("v_add_f32 %0, %0, %2\n\tv_mul_f32 %0, 0xbb124925, %0\n\tv_cvt_pk_bf16_f32 %1, %0, %0" : "+v"(mt0), "=&v"(mtp) : "v"(mt1));
  bext[0] = mtp;
  SB(); GM();
  MF32("a", acc[1], wg[20], xf[0]);
  DSRTR(xf[5], xadr[1], 16384, 16640);
  SB(); GM();
  WAITL(8);
  MF32("a", acc[1], wg[21], xf[1]);
  DSRTR(xf[6], xadr[1], 20480, 20736);
  asm volatile("v_add_u32 %0, %1, %2" : "=v"(xadr[0]) : "s"(sXg[0] + 114688u), "v"(xl0));
  SB(); GM();
  WAITL(8);
  MF32("a", acc[1], wg[22], xf[2]);
  DSRTR(xf[7], xadr[1], 24576, 24832);
  SB(); GM();
  WAITL(8);
  MF32("a", acc[1], wg[23], xf[3]);
  WAITV(15);
  SBAR();
  DSRTR(xf[0], xadr[0], 0, 256);
  SB(); GM();
  WAITL(8);
  MF32("a", acc[1], wg[24], xf[4]);
  DSRTR(xf[1], xadr[0], 4096, 4352);
  { const unsigned so_ = sCp + 28672u; BLOAD2(pollx, v_pc, so_); }
  BLOADWD(wd[12][1], 69632);
  SB(); GM();
  WAITL(8);
  MF32("a", acc[1], wg[25], xf[5]);
  DSRTR(xf[2], xadr[0], 8192, 8448);
  BLOADWD(wd[12][2], 126976);
  SB(); GM();
  WAITL(8);
  MF32("a", acc[1], wg[26], xf[6]);
  DSRTR(xf[3], xadr[0], 12288, 12544);
  BLOADWD(wd[13][0], 13312);
  SB(); GM();
  WAITL(8);
  MF32("a", acc[1], wg[27], xf[7]);
  DSRTR(xf[4], xadr[0], 16384, 16640);
  BLOADWD(wd[13][1], 70656);
  SB(); GM();
  MF32("v", acc[1], aext, bext);
  DSRTR(xf[5], xadr[0], 20480, 20736);
  asm volatile("v_add_u32 %0, %1, %2" : "=v"(xadr[1]) : "s"(sXg[1] + 114688u), "v"(xl0));
  SB(); GM();
  WAITL(10);
  MF32Z("v", acc[0], wg[0], xf[0]);
  BLOADWD(wd[13][2], 128000);
  MF16Z("v", sacc, sel, xf[0]);
  SB(); GM();
  WAITL(8);
  MF32("v", acc[0], wg[1], xf[1]);
  DSRTR(xf[6], xadr[0], 24576, 24832);
  MF16("v", sacc, sel, xf[1]);
  GLDSW(0, sWd, 172032);
  asm volatile("v_max_f32 %0, 0, %1" : "=v"(ep0) : "v"(acc[1][0]));
  SB(); GM();
  WAITL(8);
  MF32("v", acc[0], wg[2], xf[2]);
  DSRTR(xf[7], xadr[1], 0, 256);
  asm volatile("v_max_f32 %0, 0, %3\n\tv_mul_f32 %1, %1, %4\n\tv_mul_f32 %0, %0, %5\n\tv_cvt_pk_bf16_f32 %2, %1, %0\n\tv_max_f32 %1, 0, %6" : "=&v"(ep1), "+v"(ep0), "=&v"(hp0) : "v"(acc[1][1]), "v"(acc[1][8]), "v"(acc[1][9]), "v"(acc[1][2]));
  MF16("v", sacc, sel, xf[2]);
  GLDSW(1024, sWd, 173056);
  asm volatile("v_max_f32 %0, 0, %1" : "=v"(ep1) : "v"(acc[1][3]));
  SB(); GM();
  WAITL(8);
  MF32("v", acc[0], wg[3], xf[3]);
  DSRTR(xf[0], xadr[1], 4096, 4352);
  asm volatile("v_mul_f32 %0, %0, %3\n\tv_mul_f32 %1, %1, %4\n\tv_cvt_pk_bf16_f32 %2, %0, %1\n\tv_max_f32 %0, 0, %5\n\tv_max_f32 %1, 0, %6" : "+v"(ep0), "+v"(ep1), "=&v"(hp1) : "v"(acc[1][10]), "v"(acc[1][11]), "v"(acc[1][4]), "v"(acc[1][5]));
  MF16("v", sacc, sel, xf[3]);
  GLDSW(2048, sWd, 174080);
  asm volatile("v_mul_f32 %0, %0, %1" : "+v"(ep0) : "v"(acc[1][12]));
  SB(); GM();
  WAITL(8);
  MF32("v", acc[0], wg[4], xf[4]);
  DSRTR(xf[1], xadr[1], 8192, 8448);
  asm volatile("v_mul_f32 %0, %0, %3\n\tv_cvt_pk_bf16_f32 %1, %2, %0\n\tv_max_f32 %2, 0, %4\n\tv_max_f32 %0, 0, %5\n\tv_mul_f32 %2, %2, %6" : "+v"(ep1), "=&v"(hp2), "+v"(ep0) : "v"(acc[1][13]), "v"(acc[1][6]), "v"(acc[1][7]), "v"(acc[1][14]));
  MF16("v", sacc, sel, xf[4]);
  GLDSW(3072, sWd, 175104);
  asm volatile("v_mul_f32 %0, %0, %1" : "+v"(ep1) : "v"(acc[1][15]));
  SB(); GM();
  WAITL(8);
  MF32("v", acc[0], wg[5], xf[5]);
  DSRTR(xf[2], xadr[1], 12288, 12544);
  asm volatile("v_cvt_pk_bf16_f32 %0, %1, %2" : "=v"(hp3) : "v"(ep0), "v"(ep1));
  { hfr = v4u{hp0, hp1, hp2, hp3}; const unsigned so_ = sHst + 0 * H_LAYER + 917504u; BSTORE4(hfr, v_hst, so_); }
  MF16("v", sacc, sel, xf[5]);
  GLDSW(4096, sWd, 176128);
  SB(); GM();
  WAITL(8);
  MF32("a", acc[0], wg[6], xf[6]);
  DSRTR(xf[3], xadr[1], 16384, 16640);
  MF16("v", sacc, sel, xf[6]);
  GLDSW(5120, sWd, 177152);
  SB(); GM();
  WAITL(8);
  MF32("a", acc[0], wg[7], xf[7]);
  DSRTR(xf[4], xadr[1], 20480, 20736);
  asm volatile("v_add_u32 %0, %1, %2" : "=v"(xadr[0]) : "s"(sXg[2] + 114688u), "v"(xl0));
  SB(); GM();
  WAITL(8);
  MF32("a", acc[0], wg[8], xf[0]);
  DSRTR(xf[5], xadr[1], 24576, 24832);
  SB(); GM();
  WAITL(8);
  MF32("a", acc[0], wg[9], xf[1]);
  DSRTR(xf[6], xadr[0], 0, 256);
  WAITV(13);
  { const unsigned so_ = sHf + 0u; BSTORE2(tagv, v_one, so_); }
  GLDSW(6144, sWd, 178176);
  SB(); GM();
  WAITL(8);
  MF32("a", acc[0], wg[10], xf[2]);
  DSRTR(xf[7], xadr[0], 4096, 4352);
  GLDSW(7168, sWd, 179200);
  SB(); GM();
  WAITL(8);
  MF32("a", acc[0], wg[11], xf[3]);
  DSRTR(xf[0], xadr[0], 8192, 8448);
  { asm volatile("" :: "v"(sacc)); float s0_ = sacc[0]; DSW32(st_wr, s0_, 0); }
  DSRU16(er0, res_rd, 0);
  SB(); GM();
  WAITL(10);
  MF32("a", acc[0], wg[12], xf[4]);
  DSRTR(xf[1], xadr[0], 12288, 12544);
  GLDSW(8192, sWd, 180224);
  SB(); GM();
  WAITL(10);
  MF32("a", acc[0], wg[13], xf[5]);
  DSRTR(xf[2], xadr[0], 16384, 16640);
  WAITV(16);
  { const unsigned so_ = sCp + 28672u; CHECKF(pollx, v_pc, so_); }
  WAITL(5);
  SBAR();
  SB(); GM();
  MF32("a", acc[0], wg[14], xf[6]);
  DSRTR(xf[3], xadr[0], 20480, 20736);
  asm volatile("v_add_u32 %0, %1, %2" : "=v"(xadr[1]) : "s"(sXg[3] + 114688u), "v"(xl0));
  GLDS1(28672, sX1, 917504);
  SB(); GM();
  MF32("a", acc[0], wg[15], xf[7]);
  DSRTR(xf[4], xadr[0], 24576, 24832);
  DSR128(stv, st_rd, 0);
  DSRU16(ey0, res_rd, 2048);
  SB(); GM();
  MF32("a", acc[0], wg[16], xf[0]);
  DSRTR(xf[5], xadr[1], 0, 256);
  GLDS1(29696, sX1, 918528);
  SB(); GM();
  WAITL(10);
  MF32("a", acc[0], wg[17], xf[1]);
  DSRTR(xf[6], xadr[1], 4096, 4352);
  DSRU16(er1, res_rd, 512);
  SB(); GM();
  WAITL(11);
  MF32("a", acc[0], wg[18], xf[2]);
  DSRTR(xf[7], xadr[1], 8192, 8448);
  GLDS1(30720, sX1, 919552);
  SB(); GM();
  WAITL(11);
  MF32("a", acc[0], wg[19], xf[3]);
  DSRTR(xf[0], xadr[1], 12288, 12544);
  WAITL(10);
  asm volatile("v_add_f32 %0, %2, %3\n\tv_add_f32 %1, %4, %5" : "=&v"(mt0), "=&v"(mt1) : "v"(stv[0]), "v"(stv[1]), "v"(stv[2]), "v"(stv[3]));
  SB(); GM();
  MF32("a", acc[0], wg[20], xf[4]);
  DSRTR(xf[1], xadr[1], 16384, 16640);
  GLDS1(31744, sX1, 920576);
  SB(); GM();
  WAITL(9);
  MF32("a", acc[0], wg[21], xf[5]);
  DSRTR(xf[2], xadr[1], 20480, 20736);
  asm volatile("v_add_u32 %0, %1, %2" : "=v"(xadr[0]) : "s"(sXg[0] + 0u), "v"(xl0));
  asm volatile("v_add_f32 %0, %0, %1\n\tv_mul_f32 %0, 0xbb124925, %0" : "+v"(mt0) : "v"(mt1));
  SB(); GM();
  WAITL(9);
  MF32("a", acc[0], wg[22], xf[6]);
  DSRTR(xf[3], xadr[1], 24576, 24832);
  GLDS1(32768, sX1, 921600);
  SB(); GM();
  WAITL(8);
  MF32("a", acc[0], wg[23], xf[7]);
  SBAR();
  DSRTR(xf[4], xadr[0], 0, 256);
  asm volatile("v_cvt_pk_bf16_f32 %0, %1, %1" : "=v"(mtp) : "v"(mt0));
  bext[0] = mtp;
  DSRU16(ey1, res_rd, 2560);
  SB(); GM();
  WAITL(9);
  MF32("a", acc[0], wg[24], xf[0]);
  DSRTR(xf[5], xadr[0], 4096, 4352);
  GLDS1(33792, sX1, 922624);
  SB(); GM();
  WAITL(9);
  MF32("a", acc[0], wg[25], xf[1]);
  DSRTR(xf[6], xadr[0], 8192, 8448);
  DSRU16(er2, res_rd, 1024);
  SB(); GM();
  WAITL(10);
  MF32("a", acc[0], wg[26], xf[2]);
  DSRTR(xf[7], xadr[0], 12288, 12544);
  GLDS1(34816, sX1, 923648);
  SB(); GM();
  WAITL(10);
  MF32("a", acc[0], wg[27], xf[3]);
  DSRTR(xf[0], xadr[0], 16384, 16640);
  DSRU16(ey2, res_rd, 3072);
  SB(); GM();
  MF32("v", acc[0], aext, bext);
  DSRTR(xf[1], xadr[0], 20480, 20736);
  asm volatile("v_add_u32 %0, %1, %2" : "=v"(xadr[1]) : "s"(sXg[1] + 0u), "v"(xl0));
  GLDSR(8192, 917504);
  SB(); GM();
  WAITL(13);
  MF32Z("v", acc[1], wg[0], xf[4]);
  DSRU16(er3, res_rd, 1536);
  MF16Z("v", sacc, sel, xf[4]);
  SBAR();
  DSRU16(ey3, res_rd, 3584);
  SB(); GM();
  WAITL(12);
  MF32("v", acc[1], wg[1], xf[5]);
  DSRTR(xf[2], xadr[0], 24576, 24832);
  MF16("v", sacc, sel, xf[5]);
  asm volatile("v_max_f32 %0, 0, %2\n\tv_max_f32 %1, 0, %3" : "=&v"(ep0), "=&v"(ep1) : "v"(acc[0][0]), "v"(acc[0][1]));
  SB(); GM();
  WAITL(12);
  MF32("v", acc[1], wg[2], xf[6]);
  DSRTR(xf[3], xadr[1], 0, 256);
  GLDSR(12288, 1376256);
  MF16("v", sacc, sel, xf[6]);
  asm volatile("v_mul_f32 %0, %0, %2\n\tv_mul_f32 %1, %1, %3" : "+v"(ep0), "+v"(ep1) : "v"(acc[0][8]), "v"(acc[0][9]));
  SB(); GM();
  WAITL(11);
  MF32("v", acc[1], wg[3], xf[7]);
  DSRTR(xf[4], xadr[1], 4096, 4352);
  asm volatile("v_cvt_pk_bf16_f32 %0, %1, %2\n\tv_max_f32 %1, 0, %3\n\tv_max_f32 %2, 0, %4" : "=&v"(hp0), "+v"(ep0), "+v"(ep1) : "v"(acc[0][2]), "v"(acc[0][3]));
  MF16("v", sacc, sel, xf[7]);
  asm volatile("v_mul_f32 %0, %0, %2\n\tv_mul_f32 %1, %1, %3" : "+v"(ep0), "+v"(ep1) : "v"(acc[0][10]), "v"(acc[0][11]));
  SB(); GM();
  WAITL(11);
  MF32("v", acc[1], wg[4], xf[0]);
  DSRTR(xf[5], xadr[1], 8192, 8448);
  asm volatile("v_cvt_pk_bf16_f32 %0, %1, %2\n\tv_max_f32 %1, 0, %3\n\tv_max_f32 %2, 0, %4" : "=&v"(hp1), "+v"(ep0), "+v"(ep1) : "v"(acc[0][4]), "v"(acc[0][5]));
  MF16("v", sacc, sel, xf[0]);
  GLDS1(114688, sX1, 1146880);
  SB(); GM();
  WAITL(10);
  MF32("v", acc[1], wg[5], xf[1]);
  DSRTR(xf[6], xadr[1], 12288, 12544);
  asm volatile("v_mul_f32 %0, %0, %3\n\tv_mul_f32 %1, %1, %4\n\tv_cvt_pk_bf16_f32 %2, %0, %1" : "+v"(ep0), "+v"(ep1), "=&v"(hp2) : "v"(acc[0][12]), "v"(acc[0][13]));
  MF16("v", sacc, sel, xf[1]);
  GLDS1(115712, sX1, 1147904);
  SB(); GM();
  WAITL(8);
  MF32("a", acc[1], wg[6], xf[2]);
  DSRTR(xf[7], xadr[1], 16384, 16640);
  asm volatile("v_max_f32 %0, 0, %2\n\tv_max_f32 %1, 0, %3\n\tv_mul_f32 %0, %0, %4" : "=&v"(ep0), "=&v"(ep1) : "v"(acc[0][6]), "v"(acc[0][7]), "v"(acc[0][14]));
  MF16("v", sacc, sel, xf[2]);
  asm volatile("v_mul_f32 %0, %0, %2\n\tv_cvt_pk_bf16_f32 %1, %3, %0" : "+v"(ep1), "=&v"(hp3) : "v"(acc[0][15]), "v"(ep0));
  SB(); GM();
  WAITL(8);
  MF32("a", acc[1], wg[7], xf[3]);
  DSRTR(xf[0], xadr[1], 20480, 20736);
  asm volatile("v_add_u32 %0, %1, %2" : "=v"(xadr[0]) : "s"(sXg[2] + 0u), "v"(xl0));
  GLDS1(116736, sX1, 1148928);
  SB(); GM();
  WAITL(8);
  MF32("a", acc[1], wg[8], xf[4]);
  DSRTR(xf[1], xadr[1], 24576, 24832);
  { hfr = v4u{hp0, hp1, hp2, hp3}; const unsigned so_ = sHst + 0 * H_LAYER + 1835008u; BSTORE4(hfr, v_hst, so_); }
  SB(); GM();
  WAITL(8);
  MF32("a", acc[1], wg[9], xf[5]);
  DSRTR(xf[2], xadr[0], 0, 256);
  GLDS1(117760, sX1, 1149952);
  SB(); GM();
  WAITL(8);
  MF32("a", acc[1], wg[10], xf[6]);
  DSRTR(xf[3], xadr[0], 4096, 4352);
  { asm volatile("" :: "v"(sacc)); float s0_ = sacc[0]; DSW32(st_wr, s0_, 512); }
  asm volatile("v_lshl_or_b32 %0, %2, 16, %1" : "=v"(rp0_0) : "v"(er0), "v"(er1));
  SB(); STUB(10, ".Lwret10"); GM();
  WAITL(9);
  MF32("a", acc[1], wg[11], xf[7]);
  DSRTR(xf[4], xadr[0], 8192, 8448);
  GLDS1(118784, sX1, 1150976);
  SB(); GM();
  WAITL(9);
  MF32("a", acc[1], wg[12], xf[0]);
  DSRTR(xf[5], xadr[0], 12288, 12544);
  asm volatile("v_lshl_or_b32 %0, %4, 16, %3\n\tv_lshl_or_b32 %1, %6, 16, %5\n\tv_lshl_or_b32 %2, %8, 16, %7" : "=&v"(rp0_1), "=&v"(yp0_0), "=&v"(yp0_1) : "v"(er2), "v"(er3), "v"(ey0), "v"(ey1), "v"(ey2), "v"(ey3));
  SB(); GM();
  WAITL(9);
  MF32("a", acc[1], wg[13], xf[1]);
  DSRTR(xf[6], xadr[0], 16384, 16640);
  GLDS1(119808, sX1, 1152000);
  SB(); GM();
  WAITL(9);
  MF32("a", acc[1], wg[14], xf[2]);
  DSRTR(xf[7], xadr[0], 20480, 20736);
  asm volatile("v_add_u32 %0, %1, %2" : "=v"(xadr[1]) : "s"(sXg[3] + 0u), "v"(xl0));
  asm volatile("v_lshlrev_b32 %0, 16, %0\n\tv_lshlrev_b32 %1, 16, %1" : "+v"(er0), "+v"(ey0) : );
  SB(); GM();
  WAITL(9);
  MF32("a", acc[1], wg[15], xf[3]);
  DSRTR(xf[0], xadr[0], 24576, 24832);
  WAITV(22);
  { const unsigned so_ = sHf + 114688u; BSTORE2(tagv, v_one, so_); }
  GLDS1(120832, sX1, 1153024);
  SB(); GM();
  WAITL(8);
  MF32("a", acc[1], wg[16], xf[4]);
  DSRTR(xf[1], xadr[1], 0, 256);
  GLDSW(9216, sWd, 181248);
  SB(); GM();
  WAITL(8);
  MF32("a", acc[1], wg[17], xf[5]);
  DSRTR(xf[2], xadr[1], 4096, 4352);
  SBAR();
  DSR128(stv, st_rd, 512);
  SB(); GM();
  WAITL(9);
  MF32("a", acc[1], wg[18], xf[6]);
  DSRTR(xf[3], xadr[1], 8192, 8448);
  GLDSW(10240, sWd, 182272);
  SB(); GM();
  WAITL(9);
  MF32("a", acc[1], wg[19], xf[7]);
  DSRTR(xf[4], xadr[1], 12288, 12544);
  asm volatile("v_lshlrev_b32 %0, 16, %0\n\tv_lshlrev_b32 %1, 16, %1\n\tv_lshlrev_b32 %2, 16, %2" : "+v"(er1), "+v"(ey1), "+v"(er2) : );
  SB(); GM();
  WAITL(9);
  MF32("a", acc[1], wg[20], xf[0]);
  DSRTR(xf[5], xadr[1], 16384, 16640);
  GLDSW(11264, sWd, 183296);
  SB(); GM();
  WAITL(9);
  MF32("a", acc[1], wg[21], xf[1]);
  DSRTR(xf[6], xadr[1], 20480, 20736);
  asm volatile("v_add_u32 %0, %1, %2" : "=v"(xadr[0]) : "s"(sXg[0] + 28672u), "v"(xl0));
  WAITL(8);
  asm volatile("v_add_f32 %0, %1, %2" : "=v"(mt0) : "v"(stv[0]), "v"(stv[1]));
  SB(); GM();
  MF32("a", acc[1], wg[22], xf[2]);
  DSRTR(xf[7], xadr[1], 24576, 24832);
  GLDSW(12288, sWd, 184320);
  SB(); GM();
  WAITL(8);
  MF32("a", acc[1], wg[23], xf[3]);
  asm volatile("v_add_f32 %0, %3, %4\n\tv_add_f32 %1, %1, %0\n\tv_mul_f32 %1, 0xbb124925, %1\n\tv_cvt_pk_bf16_f32 %2, %1, %1" : "=&v"(mt1), "+v"(mt0), "=&v"(mtp) : "v"(stv[2]), "v"(stv[3]));
  bext[0] = mtp;
  asm volatile("v_lshlrev_b32 %0, 16, %0" : "+v"(ey2) : );
  SB(); GM();
  WAITL(6);
  MF32("a", acc[1], wg[24], xf[4]);
  GLDSW(13312, sWd, 185344);
  asm volatile("v_lshlrev_b32 %0, 16, %0\n\tv_lshlrev_b32 %1, 16, %1" : "+v"(er3), "+v"(ey3) : );
  SB(); GM();
  WAITL(4);
  MF32("a", acc[1], wg[25], xf[5]);
  WAITV(16);
  SBAR();
  DSRTR(xf[0], xadr[0], 0, 256);
  DSRTR(xf[1], xadr[0], 4096, 4352);
  asm volatile("v_add_f32 %0, %1, %2" : "=v"(ea) : "v"(er0), "v"(er1));
  SB(); GM();
  WAITL(6);
  MF32("a", acc[1], wg[26], xf[6]);
  DSRTR(xf[2], xadr[0], 8192, 8448);
  asm volatile("v_mul_f32 %0, %3, %3\n\tv_add_f32 %1, %4, %5\n\tv_add_f32 %2, %2, %6" : "=&v"(eb), "=&v"(ec), "+v"(ea) : "v"(er0), "v"(ey0), "v"(ey1), "v"(er2));
  SB(); GM();
  WAITL(6);
  MF32("a", acc[1], wg[27], xf[7]);
  DSRTR(xf[3], xadr[0], 12288, 12544);
  asm volatile("v_fma_f32 %0, %3, %3, %0\n\tv_add_f32 %1, %1, %4\n\tv_add_f32 %2, %2, %5" : "+v"(eb), "+v"(ec), "+v"(ea) : "v"(er1), "v"(ey2), "v"(er3));
  SB(); GM();
  MF32("v", acc[1], aext, bext);
  DSRTR(xf[4], xadr[0], 16384, 16640);
  asm volatile("v_fma_f32 %0, %3, %3, %0\n\tv_add_f32 %1, %1, %4\n\tv_fma_f32 %0, %5, %5, %0\n\tv_add_f32_dpp %2, %6, %6 row_ror:8 row_mask:0xf bank_mask:0xf" : "+v"(eb), "+v"(ec), "=&v"(et) : "v"(er2), "v"(ey3), "v"(er3), "v"(ea));
  SB(); GM();
  WAITL(8);
  MF32Z("v", acc[0], wg[0], xf[0]);
  DSRTR(xf[5], xadr[0], 20480, 20736);
  asm volatile("v_add_u32 %0, %1, %2" : "=v"(xadr[1]) : "s"(sXg[1] + 28672u), "v"(xl0));
  asm volatile("v_add_f32_dpp %0, %0, %0 row_ror:8 row_mask:0xf bank_mask:0xf\n\tv_add_f32_dpp %1, %2, %2 row_ror:8 row_mask:0xf bank_mask:0xc" : "+v"(ec), "+v"(et) : "v"(eb));
  MF16Z("v", sacc, sel, xf[0]);
  SBAR();
  asm volatile("v_add_f32_dpp %0, %1, %1 row_half_mirror row_mask:0xf bank_mask:0xf" : "=v"(eu) : "v"(et));
  SB(); GM();
  WAITL(8);
  MF32("v", acc[0], wg[1], xf[1]);
  DSRTR(xf[6], xadr[0], 24576, 24832);
  GLDS1(0, sX1, 1376256);
  MF16("v", sacc, sel, xf[1]);
  asm volatile("v_max_f32 %0, 0, %2\n\tv_max_f32 %1, 0, %3\n\tv_mul_f32 %0, %0, %4\n\tv_mul_f32 %1, %1, %5" : "=&v"(ep0), "=&v"(ep1) : "v"(acc[1][0]), "v"(acc[1][1]), "v"(acc[1][8]), "v"(acc[1][9]));
  SB(); GM();
  WAITL(8);
  MF32("v", acc[0], wg[2], xf[2]);
  DSRTR(xf[7], xadr[1], 0, 256);
  GLDS1(1024, sX1, 1377280);
  asm volatile("v_cvt_pk_bf16_f32 %0, %1, %2\n\tv_max_f32 %1, 0, %3" : "=&v"(hp0), "+v"(ep0) : "v"(ep1), "v"(acc[1][2]));
  MF16("v", sacc, sel, xf[2]);
  asm volatile("v_max_f32 %0, 0, %3\n\tv_mul_f32 %1, %1, %4\n\tv_mul_f32 %0, %0, %5\n\tv_cvt_pk_bf16_f32 %2, %1, %0" : "=&v"(ep1), "+v"(ep0), "=&v"(hp1) : "v"(acc[1][3]), "v"(acc[1][10]), "v"(acc[1][11]));
  SB(); GM();
  WAITL(8);
  MF32("v", acc[0], wg[3], xf[3]);
  DSRTR(xf[0], xadr[1], 4096, 4352);
  GLDS1(2048, sX1, 1378304);
  asm volatile("v_max_f32 %0, 0, %2\n\tv_max_f32 %1, 0, %3" : "=&v"(ep0), "=&v"(ep1) : "v"(acc[1][4]), "v"(acc[1][5]));
  MF16("v", sacc, sel, xf[3]);
  asm volatile("v_mul_f32 %0, %0, %3\n\tv_mul_f32 %1, %1, %4\n\tv_cvt_pk_bf16_f32 %2, %0, %1\n\tv_max_f32 %0, 0, %5" : "+v"(ep0), "+v"(ep1), "=&v"(hp2) : "v"(acc[1][12]), "v"(acc[1][13]), "v"(acc[1][6]));
  SB(); GM();
  WAITL(8);
  MF32("v", acc[0], wg[4], xf[4]);
  DSRTR(xf[1], xadr[1], 8192, 8448);
  GLDS1(3072, sX1, 1379328);
  asm volatile("v_max_f32 %0, 0, %2\n\tv_mul_f32 %1, %1, %3" : "=&v"(ep1), "+v"(ep0) : "v"(acc[1][7]), "v"(acc[1][14]));
  MF16("v", sacc, sel, xf[4]);
  asm volatile("v_mul_f32 %0, %0, %2\n\tv_cvt_pk_bf16_f32 %1, %3, %0" : "+v"(ep1), "=&v"(hp3) : "v"(acc[1][15]), "v"(ep0));
  SB(); GM();
  WAITL(8);
  MF32("v", acc[0], wg[5], xf[5]);
  DSRTR(xf[2], xadr[1], 12288, 12544);
  GLDS1(4096, sX1, 1380352);
  MF16("v", sacc, sel, xf[5]);
  { hfr = v4u{hp0, hp1, hp2, hp3}; const unsigned so_ = sHst + 0 * H_LAYER + 2752512u; BSTORE4(hfr, v_hst, so_); }
  SB(); GM();
  WAITL(8);
  MF32("a", acc[0], wg[6], xf[6]);
  DSRTR(xf[3], xadr[1], 16384, 16640);
  GLDS1(5120, sX1, 1381376);
  MF16("v", sacc, sel, xf[6]);
  asm volatile("v_add_f32_dpp %0, %1, %1 row_half_mirror row_mask:0xf bank_mask:0x2\n\ts_nop 1\n\tv_add_f32_dpp %0, %0, %0 quad_perm:[1,0,3,2] row_mask:0xf bank_mask:0xf" : "+v"(eu) : "v"(ec));
  SB(); GM();
  WAITL(8);
  MF32("a", acc[0], wg[7], xf[7]);
  DSRTR(xf[4], xadr[1], 20480, 20736);
  asm volatile("v_add_u32 %0, %1, %2" : "=v"(xadr[0]) : "s"(sXg[2] + 28672u), "v"(xl0));
  GLDS1(6144, sX1, 1382400);
  SB(); GM();
  WAITL(8);
  MF32("a", acc[0], wg[8], xf[0]);
  DSRTR(xf[5], xadr[1], 24576, 24832);
  asm volatile("v_add_f32_dpp %0, %0, %0 quad_perm:[2,3,0,1] row_mask:0xf bank_mask:0xf" : "+v"(eu) : );
  { gp[0] = f2u(eu); const unsigned so_ = sGs + 0 * GR_KIND + 0u; BSTORE2(gp, v_gst, so_); }
  SB(); GM();
  WAITL(8);
  MF32("a", acc[0], wg[9], xf[1]);
  DSRTR(xf[6], xadr[0], 0, 256);
  { asm volatile("" :: "v"(sacc)); float s0_ = sacc[0]; DSW32(st_wr, s0_, 0); }
  DSRU16(er0, res_rd, 4096);
  SB(); GM();
  WAITL(10);
  MF32("a", acc[0], wg[10], xf[2]);
  DSRTR(xf[7], xadr[0], 4096, 4352);
  DSRU16(ey0, res_rd, 6144);
  SB(); GM();
  WAITL(11);
  MF32("a", acc[0], wg[11], xf[3]);
  DSRTR(xf[0], xadr[0], 8192, 8448);
  DSRU16(er1, res_rd, 4608);
  SB(); GM();
  WAITL(12);
  MF32("a", acc[0], wg[12], xf[4]);
  DSRTR(xf[1], xadr[0], 12288, 12544);
  WAITV(19);
  { const unsigned so_ = sHf + 229376u; BSTORE2(tagv, v_one, so_); }
  SB(); GM();
  WAITL(12);
  MF32("a", acc[0], wg[13], xf[5]);
  DSRTR(xf[2], xadr[0], 16384, 16640);
  DSRU16(ey1, res_rd, 6656);
  SB(); GM();
  WAITL(13);
  MF32("a", acc[0], wg[14], xf[6]);
  DSRTR(xf[3], xadr[0], 20480, 20736);
  asm volatile("v_add_u32 %0, %1, %2" : "=v"(xadr[1]) : "s"(sXg[3] + 28672u), "v"(xl0));
  DSRU16(er2, res_rd, 5120);
  SB(); GM();
  WAITL(12);
  MF32("a", acc[0], wg[15], xf[7]);
  DSRTR(xf[4], xadr[0], 24576, 24832);
  SBAR();
  DSR128(stv, st_rd, 0);
  SB(); GM();
  WAITL(12);
  MF32("a", acc[0], wg[16], xf[0]);
  DSRTR(xf[5], xadr[1], 0, 256);
  DSRU16(ey2, res_rd, 7168);
  SB(); GM();
  WAITL(12);
  MF32("a", acc[0], wg[17], xf[1]);
  DSRTR(xf[6], xadr[1], 4096, 4352);
  DSRU16(er3, res_rd, 5632);
  SB(); GM();
  WAITL(13);
  MF32("a", acc[0], wg[18], xf[2]);
  DSRTR(xf[7], xadr[1], 8192, 8448);
  WAITL(8);
  asm volatile("v_add_f32 %0, %2, %3\n\tv_add_f32 %1, %4, %5" : "=&v"(mt0), "=&v"(mt1) : "v"(stv[0]), "v"(stv[1]), "v"(stv[2]), "v"(stv[3]));
  SB(); GM();
  MF32("a", acc[0], wg[19], xf[3]);
  DSRTR(xf[0], xadr[1], 12288, 12544);
  asm volatile("v_add_f32 %0, %0, %2\n\tv_mul_f32 %0, 0xbb124925, %0\n\tv_cvt_pk_bf16_f32 %1, %0, %0" : "+v"(mt0), "=&v"(mtp) : "v"(mt1));
  bext[0] = mtp;
  SB(); GM();
  MF32("a", acc[0], wg[20], xf[4]);
  DSRTR(xf[1], xadr[1], 16384, 16640);
  DSRU16(ey3, res_rd, 7680);
  SB(); GM();
  WAITL(11);
  MF32("a", acc[0], wg[21], xf[5]);
  DSRTR(xf[2], xadr[1], 20480, 20736);
  asm volatile("v_add_u32 %0, %1, %2" : "=v"(xadr[0]) : "s"(sXg[0] + 114688u), "v"(xl0));
  SB(); GM();
  WAITL(10);
  MF32("a", acc[0], wg[22], xf[6]);
  DSRTR(xf[3], xadr[1], 24576, 24832);
  SB(); GM();
  WAITL(9);
  MF32("a", acc[0], wg[23], xf[7]);
  WAITV(15);
  SBAR();
  DSRTR(xf[4], xadr[0], 0, 256);
  WAITL(6);
  SB(); GM();
  MF32("a", acc[0], wg[24], xf[0]);
  DSRTR(xf[5], xadr[0], 4096, 4352);
  asm volatile("v_lshl_or_b32 %0, %4, 16, %3\n\tv_lshl_or_b32 %1, %6, 16, %5\n\tv_lshl_or_b32 %2, %8, 16, %7" : "=&v"(rp1_0), "=&v"(rp1_1), "=&v"(yp1_0) : "v"(er0), "v"(er1), "v"(er2), "v"(er3), "v"(ey0), "v"(ey1));
  SB(); GM();
  MF32("a", acc[0], wg[25], xf[1]);
  DSRTR(xf[6], xadr[0], 8192, 8448);
  asm volatile("v_lshl_or_b32 %0, %4, 16, %3\n\tv_lshlrev_b32 %1, 16, %1\n\tv_lshlrev_b32 %2, 16, %2" : "=&v"(yp1_1), "+v"(er0), "+v"(ey0) : "v"(ey2), "v"(ey3));
  SB(); GM();
  WAITL(8);
  MF32("a", acc[0], wg[26], xf[2]);
  DSRTR(xf[7], xadr[0], 12288, 12544);
  asm volatile("v_lshlrev_b32 %0, 16, %0\n\tv_lshlrev_b32 %1, 16, %1\n\tv_lshlrev_b32 %2, 16, %2" : "+v"(er1), "+v"(ey1), "+v"(er2) : );
  SB(); GM();
  WAITL(8);
  MF32("a", acc[0], wg[27], xf[3]);
  DSRTR(xf[0], xadr[0], 16384, 16640);
  asm volatile("v_lshlrev_b32 %0, 16, %0\n\tv_lshlrev_b32 %1, 16, %1\n\tv_lshlrev_b32 %2, 16, %2" : "+v"(ey2), "+v"(er3), "+v"(ey3) : );
  SB(); GM();
  MF32("v", acc[0], aext, bext);
  DSRTR(xf[1], xadr[0], 20480, 20736);
  asm volatile("v_add_u32 %0, %1, %2" : "=v"(xadr[1]) : "s"(sXg[1] + 114688u), "v"(xl0));
  asm volatile("v_add_f32 %0, %3, %4\n\tv_mul_f32 %1, %3, %3\n\tv_add_f32 %2, %5, %6" : "=&v"(ea), "=&v"(eb), "=&v"(ec) : "v"(er0), "v"(er1), "v"(ey0), "v"(ey1));
  SB(); GM();
  WAITL(10);
  MF32Z("v", acc[1], wg[0], xf[4]);
  asm volatile("v_add_f32 %0, %0, %3\n\tv_fma_f32 %1, %4, %4, %1\n\tv_add_f32 %2, %2, %5\n\tv_add_f32 %0, %0, %6\n\tv_fma_f32 %1, %3, %3, %1" : "+v"(ea), "+v"(eb), "+v"(ec) : "v"(er2), "v"(er1), "v"(ey2), "v"(er3));
  MF16Z("v", sacc, sel, xf[4]);
  SBAR();
  asm volatile("v_add_f32 %0, %0, %1" : "+v"(ec) : "v"(ey3));
  SB(); GM();
  WAITL(8);
  MF32("v", acc[1], wg[1], xf[5]);
  DSRTR(xf[2], xadr[0], 24576, 24832);
  GLDS1(28672, sX1, 1605632);
  MF16("v", sacc, sel, xf[5]);
  { const unsigned so_ = sHp + 0u; BLOAD2(pollh, v_ph, so_); }
  SB(); GM();
  WAITL(8);
  MF32("v", acc[1], wg[2], xf[6]);
  DSRTR(xf[3], xadr[1], 0, 256);
  GLDS1(29696, sX1, 1606656);
  MF16("v", sacc, sel, xf[6]);
  asm volatile("v_max_f32 %0, 0, %2\n\tv_max_f32 %1, 0, %3" : "=&v"(ep0), "=&v"(ep1) : "v"(acc[0][0]), "v"(acc[0][1]));
  SB(); GM();
  WAITL(8);
  MF32("v", acc[1], wg[3], xf[7]);
  DSRTR(xf[4], xadr[1], 4096, 4352);
  GLDS1(30720, sX1, 1607680);
  MF16("v", sacc, sel, xf[7]);
  asm volatile("v_mul_f32 %0, %0, %2\n\tv_mul_f32 %1, %1, %3" : "+v"(ep0), "+v"(ep1) : "v"(acc[0][8]), "v"(acc[0][9]));
  SB(); GM();
  WAITL(8);
  MF32("v", acc[1], wg[4], xf[0]);
  DSRTR(xf[5], xadr[1], 8192, 8448);
  GLDS1(31744, sX1, 1608704);
  MF16("v", sacc, sel, xf[0]);
  WAITV(9);
  { const unsigned so_ = sHf + 344064u; BSTORE2(tagv, v_one, so_); }
  SB(); GM();
  WAITL(8);
  MF32("v", acc[1], wg[5], xf[1]);
  DSRTR(xf[6], xadr[1], 12288, 12544);
  GLDS1(32768, sX1, 1609728);
  MF16("v", sacc, sel, xf[1]);
  asm volatile("v_cvt_pk_bf16_f32 %0, %1, %2\n\tv_max_f32 %1, 0, %3" : "=&v"(hp0), "+v"(ep0) : "v"(ep1), "v"(acc[0][2]));
  SB(); GM();
  WAITL(8);
  MF32("a", acc[1], wg[6], xf[2]);
  DSRTR(xf[7], xadr[1], 16384, 16640);
  GLDS1(33792, sX1, 1610752);
  MF16("v", sacc, sel, xf[2]);
  asm volatile("v_max_f32 %0, 0, %2\n\tv_mul_f32 %1, %1, %3" : "=&v"(ep1), "+v"(ep0) : "v"(acc[0][3]), "v"(acc[0][10]));
  SB(); GM();
  WAITL(8);
  MF32("a", acc[1], wg[7], xf[3]);
  DSRTR(xf[0], xadr[1], 20480, 20736);
  asm volatile("v_add_u32 %0, %1, %2" : "=v"(xadr[0]) : "s"(sXg[2] + 114688u), "v"(xl0));
  GLDS1(34816, sX1, 1611776);
  SB(); GM();
  WAITL(8);
  MF32("a", acc[1], wg[8], xf[4]);
  DSRTR(xf[1], xadr[1], 24576, 24832);
  asm volatile("v_mul_f32 %0, %0, %3\n\tv_cvt_pk_bf16_f32 %1, %2, %0\n\tv_max_f32 %2, 0, %4" : "+v"(ep1), "=&v"(hp1), "+v"(ep0) : "v"(acc[0][11]), "v"(acc[0][4]));
  SB(); GM();
  WAITL(8);
  MF32("a", acc[1], wg[9], xf[5]);
  DSRTR(xf[2], xadr[0], 0, 256);
  { asm volatile("" :: "v"(sacc)); float s0_ = sacc[0]; DSW32(st_wr, s0_, 512); }
  asm volatile("v_max_f32 %0, 0, %2\n\tv_mul_f32 %1, %1, %3" : "=&v"(ep1), "+v"(ep0) : "v"(acc[0][5]), "v"(acc[0][12]));
  SB(); GM();
  WAITL(9);
  MF32("a", acc[1], wg[10], xf[6]);
  DSRTR(xf[3], xadr[0], 4096, 4352);
  asm volatile("v_mul_f32 %0, %0, %3\n\tv_cvt_pk_bf16_f32 %1, %2, %0\n\tv_max_f32 %2, 0, %4" : "+v"(ep1), "=&v"(hp2), "+v"(ep0) : "v"(acc[0][13]), "v"(acc[0][6]));
  SB(); GM();
  WAITL(9);
  MF32("a", acc[1], wg[11], xf[7]);
  DSRTR(xf[4], xadr[0], 8192, 8448);
  asm volatile("v_max_f32 %0, 0, %2\n\tv_mul_f32 %1, %1, %3\n\tv_mul_f32 %0, %0, %4" : "=&v"(ep1), "+v"(ep0) : "v"(acc[0][7]), "v"(acc[0][14]), "v"(acc[0][15]));
  SB(); GM();
  WAITL(9);
  MF32("a", acc[1], wg[12], xf[0]);
  DSRTR(xf[5], xadr[0], 12288, 12544);
  asm volatile("v_cvt_pk_bf16_f32 %0, %3, %4\n\tv_fma_f32 %1, %5, %5, %1\n\tv_add_f32_dpp %2, %6, %6 row_ror:8 row_mask:0xf bank_mask:0xf" : "=&v"(hp3), "+v"(eb), "=&v"(et) : "v"(ep0), "v"(ep1), "v"(er3), "v"(ea));
  SB(); GM();
  WAITL(9);
  MF32("a", acc[1], wg[13], xf[1]);
  DSRTR(xf[6], xadr[0], 16384, 16640);
  { hfr = v4u{hp0, hp1, hp2, hp3}; const unsigned so_ = sHst + 0 * H_LAYER + 3670016u; BSTORE4(hfr, v_hst, so_); }
  SB(); GM();
  WAITL(9);
  MF32("a", acc[1], wg[14], xf[2]);
  DSRTR(xf[7], xadr[0], 20480, 20736);
  asm volatile("v_add_u32 %0, %1, %2" : "=v"(xadr[1]) : "s"(sXg[3] + 114688u), "v"(xl0));
  asm volatile("v_add_f32_dpp %0, %0, %0 row_ror:8 row_mask:0xf bank_mask:0xf\n\tv_add_f32_dpp %1, %2, %2 row_ror:8 row_mask:0xf bank_mask:0xc" : "+v"(ec), "+v"(et) : "v"(eb));
  SB(); GM();
  WAITL(8);
  MF32("a", acc[1], wg[15], xf[3]);
  DSRTR(xf[0], xadr[0], 24576, 24832);
  SBAR();
  DSR128(stv, st_rd, 512);
  SB(); GM();
  WAITL(9);
  MF32("a", acc[1], wg[16], xf[4]);
  DSRTR(xf[1], xadr[1], 0, 256);
  asm volatile("v_add_f32_dpp %0, %1, %1 row_half_mirror row_mask:0xf bank_mask:0xf\n\tv_add_f32_dpp %0, %2, %2 row_half_mirror row_mask:0xf bank_mask:0x2\n\ts_nop 1\n\tv_add_f32_dpp %0, %0, %0 quad_perm:[1,0,3,2] row_mask:0xf bank_mask:0xf" : "=&v"(eu) : "v"(et), "v"(ec));
  SB(); GM();
  WAITL(9);
  MF32("a", acc[1], wg[17], xf[5]);
  DSRTR(xf[2], xadr[1], 4096, 4352);
  asm volatile("v_add_f32_dpp %0, %0, %0 quad_perm:[2,3,0,1] row_mask:0xf bank_mask:0xf" : "+v"(eu) : );
  { gp[0] = f2u(eu); const unsigned so_ = sGs + 0 * GR_KIND + 114688u; BSTORE2(gp, v_gst, so_); }
  SB(); GM();
  WAITL(9);
  MF32("a", acc[1], wg[18], xf[6]);
  DSRTR(xf[3], xadr[1], 8192, 8448);
  WAITL(6);
  asm volatile("v_add_f32 %0, %2, %3\n\tv_add_f32 %1, %4, %5" : "=&v"(mt0), "=&v"(mt1) : "v"(stv[0]), "v"(stv[1]), "v"(stv[2]), "v"(stv[3]));
  SB(); GM();
  MF32("a", acc[1], wg[19], xf[7]);
  DSRTR(xf[4], xadr[1], 12288, 12544);
  asm volatile("v_add_f32 %0, %0, %2\n\tv_mul_f32 %0, 0xbb124925, %0\n\tv_cvt_pk_bf16_f32 %1, %0, %0" : "+v"(mt0), "=&v"(mtp) : "v"(mt1));
  bext[0] = mtp;
  SB(); GM();
  MF32("a", acc[1], wg[20], xf[0]);
  DSRTR(xf[5], xadr[1], 16384, 16640);
  DSRU16(er0, res_rd, 8192);
  SB(); GM();
  WAITL(9);
  MF32("a", acc[1], wg[21], xf[1]);
  DSRTR(xf[6], xadr[1], 20480, 20736);
  asm volatile("v_add_u32 %0, %1, %2" : "=v"(xadr[0]) : "s"(sXg[0] + 0u), "v"(xl0));
  DSRU16(ey0, res_rd, 10240);
  SB(); GM();
  WAITL(10);
  MF32("a", acc[1], wg[22], xf[2]);
  DSRTR(xf[7], xadr[1], 24576, 24832);
  DSRU16(er1, res_rd, 8704);
  SB(); GM();
  WAITL(11);
  MF32("a", acc[1], wg[23], xf[3]);
  WAITV(13);
  SBAR();
  DSRTR(xf[0], xadr[0], 0, 256);
  WAITV(9);
  { const unsigned so_ = sHp + 0u; CHECKF(pollh, v_ph, so_); }
  { const unsigned so_ = sHl + 0 * H_LAYER + 0u; BLOAD4(H[0], v_l16, so_, 0); }
  SB(); GM();
  WAITL(11);
  MF32("a", acc[1], wg[24], xf[4]);
  DSRTR(xf[1], xadr[0], 4096, 4352);
  { const unsigned so_ = sHl + 0 * H_LAYER + 0u; BLOAD4(H[1], v_l16, so_, 1024); }
  { const unsigned so_ = sHl + 0 * H_LAYER + 0u; BLOAD4(H[2], v_l16, so_, 2048); }
  DSRU16(ey1, res_rd, 10752);
  SB(); GM();
  WAITL(12);
  MF32("a", acc[1], wg[25], xf[5]);
  DSRTR(xf[2], xadr[0], 8192, 8448);
  { const unsigned so_ = sHl + 0 * H_LAYER + 0u; BLOAD4(H[3], v_l16, so_, 3072); }
  DSRU16(er2, res_rd, 9216);
  SB(); GM();
  WAITL(12);
  MF32("a", acc[1], wg[26], xf[6]);
  DSRTR(xf[3], xadr[0], 12288, 12544);
  DSRU16(ey2, res_rd, 11264);
  SB(); GM();
  WAITL(12);
  MF32("a", acc[1], wg[27], xf[7]);
  DSRTR(xf[4], xadr[0], 16384, 16640);
  DSRU16(er3, res_rd, 9728);
  SB(); GM();
  MF32("v", acc[1], aext, bext);
  DSRTR(xf[5], xadr[0], 20480, 20736);
  asm volatile("v_add_u32 %0, %1, %2" : "=v"(xadr[1]) : "s"(sXg[1] + 0u), "v"(xl0));
  { const unsigned so_ = sHl + 0 * H_LAYER + 4096u; BLOAD4(H[4], v_l16, so_, 0); }
  DSRU16(ey3, res_rd, 11776);
  SB(); GM();
  WAITL(14);
  MF32Z("v", acc[0], wg[0], xf[0]);
  MF16Z("v", sacc, sel, xf[0]);
  { const unsigned so_ = sHl + 0 * H_LAYER + 4096u; BLOAD4(H[5], v_l16, so_, 1024); }
  SB(); GM();
  WAITL(13);
  MF32("v", acc[0], wg[1], xf[1]);
  DSRTR(xf[6], xadr[0], 24576, 24832);
  MF16("v", sacc, sel, xf[1]);
  { const unsigned so_ = sHl + 0 * H_LAYER + 4096u; BLOAD4(H[6], v_l16, so_, 2048); }
  asm volatile("v_max_f32 %0, 0, %2\n\tv_max_f32 %1, 0, %3\n\tv_mul_f32 %0, %0, %4" : "=&v"(ep0), "=&v"(ep1) : "v"(acc[1][0]), "v"(acc[1][1]), "v"(acc[1][8]));
  SB(); GM();
  WAITL(12);
  MF32("v", acc[0], wg[2], xf[2]);
  DSRTR(xf[7], xadr[1], 0, 256);
  asm volatile("v_mul_f32 %0, %0, %3\n\tv_cvt_pk_bf16_f32 %1, %2, %0\n\tv_max_f32 %2, 0, %4\n\tv_max_f32 %0, 0, %5\n\tv_mul_f32 %2, %2, %6" : "+v"(ep1), "=&v"(hp0), "+v"(ep0) : "v"(acc[1][9]), "v"(acc[1][2]), "v"(acc[1][3]), "v"(acc[1][10]));
  MF16("v", sacc, sel, xf[2]);
  { const unsigned so_ = sHl + 0 * H_LAYER + 4096u; BLOAD4(H[7], v_l16, so_, 3072); }
  asm volatile("v_mul_f32 %0, %0, %3\n\tv_cvt_pk_bf16_f32 %1, %2, %0\n\tv_max_f32 %2, 0, %4" : "+v"(ep1), "=&v"(hp1), "+v"(ep0) : "v"(acc[1][11]), "v"(acc[1][4]));
  SB(); GM();
  WAITL(11);
  MF32("v", acc[0], wg[3], xf[3]);
  DSRTR(xf[0], xadr[1], 4096, 4352);
  asm volatile("v_max_f32 %0, 0, %3\n\tv_mul_f32 %1, %1, %4\n\tv_mul_f32 %0, %0, %5\n\tv_cvt_pk_bf16_f32 %2, %1, %0\n\tv_max_f32 %1, 0, %6" : "=&v"(ep1), "+v"(ep0), "=&v"(hp2) : "v"(acc[1][5]), "v"(acc[1][12]), "v"(acc[1][13]), "v"(acc[1][6]));
  MF16("v", sacc, sel, xf[3]);
  WAITV(9);
  { const unsigned so_ = sHf + 458752u; BSTORE2(tagv, v_one, so_); }
  { const unsigned so_ = sHl + 0 * H_LAYER + 8192u; BLOAD4(H[8], v_l16, so_, 0); }
  SB(); STUB(11, ".Lwret11"); GM();
  WAITL(10);
  MF32("v", acc[0], wg[4], xf[4]);
  DSRTR(xf[1], xadr[1], 8192, 8448);
  asm volatile("v_max_f32 %0, 0, %3\n\tv_mul_f32 %1, %1, %4\n\tv_mul_f32 %0, %0, %5\n\tv_cvt_pk_bf16_f32 %2, %1, %0" : "=&v"(ep1), "+v"(ep0), "=&v"(hp3) : "v"(acc[1][7]), "v"(acc[1][14]), "v"(acc[1][15]));
  MF16("v", sacc, sel, xf[4]);
  { const unsigned so_ = sHl + 0 * H_LAYER + 8192u; BLOAD4(H[9], v_l16, so_, 1024); }
  { hfr = v4u{hp0, hp1, hp2, hp3}; const unsigned so_ = sHst + 0 * H_LAYER + 4587520u; BSTORE4(hfr, v_hst, so_); }
  SB(); GM();
  WAITL(9);
  MF32("v", acc[0], wg[5], xf[5]);
  DSRTR(xf[2], xadr[1], 12288, 12544);
  { const unsigned so_ = sGl + 0 * GR_KIND + 0u; GLOADG(gA, 244, 247, so_); }
  WAITL(10);
  MF16("v", sacc, sel, xf[5]);
  { const unsigned so_ = sHl + 0 * H_LAYER + 8192u; BLOAD4(H[10], v_l16, so_, 2048); }
  asm volatile("v_lshl_or_b32 %0, %2, 16, %1" : "=v"(rp2_0) : "v"(er0), "v"(er1));
  SB(); GM();
  WAITL(8);
  MF32("a", acc[0], wg[6], xf[6]);
  DSRTR(xf[3], xadr[1], 16384, 16640);
  asm volatile("v_lshl_or_b32 %0, %4, 16, %3\n\tv_lshl_or_b32 %1, %6, 16, %5\n\tv_lshl_or_b32 %2, %8, 16, %7" : "=&v"(rp2_1), "=&v"(yp2_0), "=&v"(yp2_1) : "v"(er2), "v"(er3), "v"(ey0), "v"(ey1), "v"(ey2), "v"(ey3));
  MF16("v", sacc, sel, xf[6]);
  asm volatile("v_lshlrev_b32 %0, 16, %0\n\tv_lshlrev_b32 %1, 16, %1" : "+v"(er0), "+v"(ey0) : );
  SB(); GM();
  WAITL(8);
  MF32("a", acc[0], wg[7], xf[7]);
  DSRTR(xf[4], xadr[1], 20480, 20736);
  asm volatile("v_add_u32 %0, %1, %2" : "=v"(xadr[0]) : "s"(sXg[2] + 0u), "v"(xl0));
  { const unsigned so_ = sHl + 0 * H_LAYER + 8192u; BLOAD4(H[11], v_l16, so_, 3072); }
  asm volatile("v_lshlrev_b32 %0, 16, %0" : "+v"(er1) : );
  SB(); GM();
  WAITL(8);
  MF32("a", acc[0], wg[8], xf[0]);
  DSRTR(xf[5], xadr[1], 24576, 24832);
  asm volatile("v_lshlrev_b32 %0, 16, %0\n\tv_lshlrev_b32 %1, 16, %1\n\tv_lshlrev_b32 %2, 16, %2" : "+v"(ey1), "+v"(er2), "+v"(ey2) : );
  SB(); GM();
  WAITL(8);
  MF32("a", acc[0], wg[9], xf[1]);
  DSRTR(xf[6], xadr[0], 0, 256);
  { const unsigned so_ = sHl + 0 * H_LAYER + 12288u; BLOAD4(H[12], v_l16, so_, 0); }
  { asm volatile("" :: "v"(sacc)); float s0_ = sacc[0]; DSW32(st_wr, s0_, 0); }
  SB(); GM();
  WAITL(9);
  MF32("a", acc[0], wg[10], xf[2]);
  DSRTR(xf[7], xadr[0], 4096, 4352);
  asm volatile("v_lshlrev_b32 %0, 16, %0\n\tv_lshlrev_b32 %1, 16, %1\n\tv_add_f32 %2, %3, %4" : "+v"(er3), "+v"(ey3), "=&v"(ea) : "v"(er0), "v"(er1));
  SB(); GM();
  WAITL(9);
  MF32("a", acc[0], wg[11], xf[3]);
  DSRTR(xf[0], xadr[0], 8192, 8448);
  { const unsigned so_ = sHl + 0 * H_LAYER + 12288u; BLOAD4(H[13], v_l16, so_, 1024); }
  asm volatile("v_mul_f32 %0, %2, %2\n\tv_add_f32 %1, %3, %4" : "=&v"(eb), "=&v"(ec) : "v"(er0), "v"(ey0), "v"(ey1));
  SB(); GM();
  WAITL(9);
  MF32("a", acc[0], wg[12], xf[4]);
  DSRTR(xf[1], xadr[0], 12288, 12544);
  asm volatile("v_add_f32 %0, %0, %3\n\tv_fma_f32 %1, %4, %4, %1\n\tv_add_f32 %2, %2, %5" : "+v"(ea), "+v"(eb), "+v"(ec) : "v"(er2), "v"(er1), "v"(ey2));
  SB(); GM();
  WAITL(9);
  MF32("a", acc[0], wg[13], xf[5]);
  DSRTR(xf[2], xadr[0], 16384, 16640);
  asm volatile("v_add_f32 %0, %0, %3\n\tv_fma_f32 %1, %4, %4, %1\n\tv_add_f32 %2, %2, %5" : "+v"(ea), "+v"(eb), "+v"(ec) : "v"(er3), "v"(er2), "v"(ey3));
  SB(); GM();
  WAITL(9);
  MF32("a", acc[0], wg[14], xf[6]);
  DSRTR(xf[3], xadr[0], 20480, 20736);
  asm volatile("v_add_u32 %0, %1, %2" : "=v"(xadr[1]) : "s"(sXg[3] + 0u), "v"(xl0));
  asm volatile("v_fma_f32 %0, %2, %2, %0\n\tv_add_f32_dpp %1, %3, %3 row_ror:8 row_mask:0xf bank_mask:0xf" : "+v"(eb), "=&v"(et) : "v"(er3), "v"(ea));
  SB(); GM();
  WAITL(8);
  MF32("a", acc[0], wg[15], xf[7]);
  DSRTR(xf[4], xadr[0], 24576, 24832);
  SBAR();
  DSR128(stv, st_rd, 0);
  SB(); GM();
  WAITL(9);
  MF32("a", acc[0], wg[16], xf[0]);
  DSRTR(xf[5], xadr[1], 0, 256);
  asm volatile("v_add_f32_dpp %0, %0, %0 row_ror:8 row_mask:0xf bank_mask:0xf\n\tv_add_f32_dpp %1, %3, %3 row_ror:8 row_mask:0xf bank_mask:0xc\n\ts_nop 1\n\tv_add_f32_dpp %2, %1, %1 row_half_mirror row_mask:0xf bank_mask:0xf" : "+v"(ec), "+v"(et), "=&v"(eu) : "v"(eb));
  SB(); GM();
  WAITL(9);
  MF32("a", acc[0], wg[17], xf[1]);
  DSRTR(xf[6], xadr[1], 4096, 4352);
  asm volatile("v_add_f32_dpp %0, %1, %1 row_half_mirror row_mask:0xf bank_mask:0x2\n\ts_nop 1\n\tv_add_f32_dpp %0, %0, %0 quad_perm:[1,0,3,2] row_mask:0xf bank_mask:0xf\n\ts_nop 1\n\tv_add_f32_dpp %0, %0, %0 quad_perm:[2,3,0,1] row_mask:0xf bank_mask:0xf" : "+v"(eu) : "v"(ec));
  SB(); GM();
  WAITL(9);
  MF32("a", acc[0], wg[18], xf[2]);
  DSRTR(xf[7], xadr[1], 8192, 8448);
  WAITL(6);
  asm volatile("v_add_f32 %0, %2, %3\n\tv_add_f32 %1, %4, %5" : "=&v"(mt0), "=&v"(mt1) : "v"(stv[0]), "v"(stv[1]), "v"(stv[2]), "v"(stv[3]));
  SB(); GM();
  MF32("a", acc[0], wg[19], xf[3]);
  DSRTR(xf[0], xadr[1], 12288, 12544);
  { const unsigned so_ = sHp + 229376u; BLOAD2(pollh, v_ph, so_); }
  asm volatile("v_add_f32 %0, %0, %1" : "+v"(mt0) : "v"(mt1));
  SB(); GM();
  MF32("a", acc[0], wg[20], xf[4]);
  DSRTR(xf[1], xadr[1], 16384, 16640);
  asm volatile("v_mul_f32 %0, 0xbb124925, %0\n\tv_cvt_pk_bf16_f32 %1, %0, %0" : "+v"(mt0), "=&v"(mtp) : );
  bext[0] = mtp;
  SB(); GM();
  WAITL(8);
  MF32("a", acc[0], wg[21], xf[5]);
  DSRTR(xf[2], xadr[1], 20480, 20736);
  asm volatile("v_add_u32 %0, %1, %2" : "=v"(xadr[0]) : "s"(sXg[0] + 28672u), "v"(xl0));
  { gp[0] = f2u(eu); const unsigned so_ = sGs + 0 * GR_KIND + 229376u; BSTORE2(gp, v_gst, so_); }
  SB(); GM();
  WAITL(8);
  MF32("a", acc[0], wg[22], xf[6]);
  DSRTR(xf[3], xadr[1], 24576, 24832);
  DSRU16(er0, res_rd, 12288);
  SB(); GM();
  WAITL(9);
  MF32("a", acc[0], wg[23], xf[7]);
  DSR128(xf[4], wd_rd, 0);
  DSRU16(ey0, res_rd, 14336);
  SB(); GM();
  WAITL(9);
  MF32("a", acc[0], wg[24], xf[0]);
  DSR128(xf[5], wd_rd, 1024);
  DSRU16(er1, res_rd, 12800);
  SB(); GM();
  WAITL(9);
  MF32("a", acc[0], wg[25], xf[1]);
  DSR128(xf[6], wd_rd, 2048);
  DSRU16(ey1, res_rd, 14848);
  SB(); GM();
  WAITL(9);
  MF32("a", acc[0], wg[26], xf[2]);
  DSR128(xf[7], wd_rd, 3072);
  DSRU16(er2, res_rd, 13312);
  SB(); GM();
  WAITL(9);
  MF32("a", acc[0], wg[27], xf[3]);
  DSR128(xf[0], wd_rd, 4096);
  DSRU16(ey2, res_rd, 15360);
  SB(); GM();
  MF32("v", acc[0], aext, bext);
  DSR128(xf[1], wd_rd, 5120);
  DSRU16(er3, res_rd, 13824);
  SB(); GM();
  WAITV(18);
  MF16Z("a", acc2[0][0], wd[0][0], H[0]);
  asm volatile("v_lshlrev_b32 %0, 16, %4\n\tv_and_b32 %1, 0xffff0000, %4\n\tv_lshlrev_b32 %2, 16, %5\n\tv_and_b32 %3, 0xffff0000, %5" : "=&v"(rf0), "=&v"(rf1), "=&v"(rf2), "=&v"(rf3) : "v"(rp0_0), "v"(rp0_1));
  SB(); GM();
  MF16Z("a", acc2[0][1], wd[0][1], H[0]);
  asm volatile("v_lshlrev_b32 %0, 16, %5\n\tv_and_b32 %1, 0xffff0000, %5\n\tv_lshlrev_b32 %2, 16, %6\n\tv_and_b32 %3, 0xffff0000, %6\n\tv_xor_b32 %4, 64, %7" : "=&v"(yf0_0), "=&v"(yf0_1), "=&v"(yf0_2), "=&v"(yf0_3), "=&v"(rw1) : "v"(yp0_0), "v"(yp0_1), "v"(red_wr));
  SB(); GM();
  MF16Z("a", acc2[0][2], wd[0][2], H[0]);
  asm volatile("v_xor_b32 %0, 128, %2\n\tv_xor_b32 %1, 192, %2" : "=&v"(rw2), "=&v"(rw3) : "v"(red_wr));
  DSRU16(ey3, res_rd, 15872);
  SB(); GM();
  WAITL(12);
  MF16Z("v", acc2[0][3], xf[4], H[0]);
  SB(); GM();
  WAITV(17);
  MF16("a", acc2[0][0], wd[1][0], H[1]);
  asm volatile("v_max_f32 %0, 0, %2\n\tv_max_f32 %1, 0, %3" : "=&v"(ep0), "=&v"(ep1) : "v"(acc[0][0]), "v"(acc[0][1]));
  SB(); GM();
  MF16("a", acc2[0][1], wd[1][1], H[1]);
  asm volatile("v_mul_f32 %0, %0, %3\n\tv_mul_f32 %1, %1, %4\n\tv_cvt_pk_bf16_f32 %2, %0, %1" : "+v"(ep0), "+v"(ep1), "=&v"(hp0) : "v"(acc[0][8]), "v"(acc[0][9]));
  SB(); GM();
  MF16("a", acc2[0][2], wd[1][2], H[1]);
  WAITV(7);
  { const unsigned so_ = sHf + 573440u; BSTORE2(tagv, v_one, so_); }
  SB(); GM();
  WAITL(10);
  MF16("v", acc2[0][3], xf[5], H[1]);
  asm volatile("v_max_f32 %0, 0, %2\n\tv_max_f32 %1, 0, %3" : "=&v"(ep0), "=&v"(ep1) : "v"(acc[0][2]), "v"(acc[0][3]));
  SB(); GM();
  MF16("a", acc2[0][0], wd[2][0], H[2]);
  asm volatile("v_mul_f32 %0, %0, %3\n\tv_mul_f32 %1, %1, %4\n\tv_cvt_pk_bf16_f32 %2, %0, %1" : "+v"(ep0), "+v"(ep1), "=&v"(hp1) : "v"(acc[0][10]), "v"(acc[0][11]));
  SB(); GM();
  MF16("a", acc2[0][1], wd[2][1], H[2]);
  asm volatile("v_max_f32 %0, 0, %2\n\tv_max_f32 %1, 0, %3\n\tv_mul_f32 %0, %0, %4" : "=&v"(ep0), "=&v"(ep1) : "v"(acc[0][4]), "v"(acc[0][5]), "v"(acc[0][12]));
  SB(); GM();
  MF16("a", acc2[0][2], wd[2][2], H[2]);
  asm volatile("v_mul_f32 %0, %0, %3\n\tv_cvt_pk_bf16_f32 %1, %2, %0\n\tv_max_f32 %2, 0, %4" : "+v"(ep1), "=&v"(hp2), "+v"(ep0) : "v"(acc[0][13]), "v"(acc[0][6]));
  SB(); GM();
  WAITL(8);
  MF16("v", acc2[0][3], xf[6], H[2]);
  asm volatile("v_max_f32 %0, 0, %2\n\tv_mul_f32 %1, %1, %3" : "=&v"(ep1), "+v"(ep0) : "v"(acc[0][7]), "v"(acc[0][14]));
  SB(); GM();
  MF16("a", acc2[0][0], wd[3][0], H[3]);
  asm volatile("v_mul_f32 %0, %0, %2\n\tv_cvt_pk_bf16_f32 %1, %3, %0" : "+v"(ep1), "=&v"(hp3) : "v"(acc[0][15]), "v"(ep0));
  WAITL(0);
  SB(); GM();
  MF16("a", acc2[0][1], wd[3][1], H[3]);
  { hfr = v4u{hp0, hp1, hp2, hp3}; const unsigned so_ = sHst + 0 * H_LAYER + 5505024u; BSTORE4(hfr, v_hst, so_); }
  SBAR();
  SB(); GM();
  MF16("a", acc2[0][2], wd[3][2], H[3]);
  WAITV(8);
  { const unsigned so_ = sGl + 0 * GR_KIND + 0u; CHECKG(gA, 244, 245, 247, so_); }
  SB(); GM();
  MF16("v", acc2[0][3], xf[7], H[3]);
  asm volatile("v_add_f32_dpp %0, %2, %2 row_ror:2 row_mask:0xf bank_mask:0xf\n\tv_add_f32_dpp %1, %3, %3 row_ror:2 row_mask:0xf bank_mask:0xf\n\ts_nop 0\n\tv_add_f32_dpp %0, %0, %0 row_ror:4 row_mask:0xf bank_mask:0xf" : "=&v"(cp0), "=&v"(cq0) : "v"(gA[0]), "v"(gA[2]));
  SB(); GM();
  MF16("a", acc2[0][0], wd[4][0], H[4]);
  asm volatile("v_add_f32_dpp %0, %0, %0 row_ror:4 row_mask:0xf bank_mask:0xf\n\tv_add_f32_dpp %1, %1, %1 row_ror:8 row_mask:0xf bank_mask:0xf\n\ts_nop 0\n\tv_add_f32_dpp %0, %0, %0 row_ror:8 row_mask:0xf bank_mask:0xf" : "+v"(cq0), "+v"(cp0) : );
  SB(); GM();
  MF16("a", acc2[0][1], wd[4][1], H[4]);
  DSR128(xf[2], wd_rd, 6144);
  asm volatile("v_mul_f32_dpp %0, %2, %3 quad_perm:[0,0,2,2] row_mask:0xf bank_mask:0xf\n\tv_mul_f32_dpp %1, %2, %3 quad_perm:[1,1,3,3] row_mask:0xf bank_mask:0xf" : "=&v"(cm0), "=&v"(cv0) : "v"(cp0), "v"(vinvD));
  SB(); GM();
  MF16("a", acc2[0][2], wd[4][2], H[4]);
  asm volatile("v_mul_f32_dpp %0, %2, %3 quad_perm:[0,0,2,2] row_mask:0xf bank_mask:0xf\n\tv_fma_f32 %1, -%4, %4, %1\n\tv_max_f32 %1, 0, %1" : "=&v"(sy0), "+v"(cv0) : "v"(cq0), "v"(vinvD), "v"(cm0));
  SB(); GM();
  MF16("v", acc2[0][3], xf[0], H[4]);
  WAITV(3);
  { const unsigned so_ = sHp + 229376u; CHECKF(pollh, v_ph, so_); }
  { const unsigned so_ = sHl + 0 * H_LAYER + 1835008u; BLOAD4(H[0], v_l16, so_, 0); }
  SB(); GM();
  MF16("a", acc2[0][0], wd[5][0], H[5]);
  { const unsigned so_ = sHl + 0 * H_LAYER + 1835008u; BLOAD4(H[1], v_l16, so_, 1024); }
  { const unsigned so_ = sHl + 0 * H_LAYER + 1835008u; BLOAD4(H[2], v_l16, so_, 2048); }
  asm volatile("v_cvt_pk_bf16_f32 %0, %1, %1" : "=v"(ct0) : "v"(cv0));
  SB(); GM();
  MF16("a", acc2[0][1], wd[5][1], H[5]);
  DSR128(xf[3], wd_rd, 7168);
  asm volatile("v_lshlrev_b32 %0, 16, %1\n\tv_add_f32 %0, %2, %0" : "=&v"(cv0) : "v"(ct0), "s"(sEps));
  SB(); GM();
  MF16("a", acc2[0][2], wd[5][2], H[5]);
  asm volatile("v_cvt_pk_bf16_f32 %0, %1, %1\n\tv_lshlrev_b32 %1, 16, %0\n\tv_lshl_or_b32 %2, %4, 16, %3" : "=&v"(ct0), "+v"(cv0), "=&v"(rp3_0) : "v"(er0), "v"(er1));
  SB(); GM();
  MF16("v", acc2[0][3], xf[1], H[5]);
  { const unsigned so_ = sHl + 0 * H_LAYER + 1835008u; BLOAD4(H[3], v_l16, so_, 3072); }
  asm volatile("v_sqrt_f32 %0, %0" : "+v"(cv0) : );
  SB(); GM();
  MF16("a", acc2[0][0], wd[6][0], H[6]);
  asm volatile("v_cvt_pk_bf16_f32 %0, %1, %1\n\tv_lshlrev_b32 %1, 16, %0\n\tv_lshl_or_b32 %2, %4, 16, %3" : "=&v"(ct0), "+v"(cv0), "=&v"(rp3_1) : "v"(er2), "v"(er3));
  SB(); GM();
  MF16("a", acc2[0][1], wd[6][1], H[6]);
  DSR128(xf[4], wd_rd, 8192);
  asm volatile("v_rcp_f32 %0, %0" : "+v"(cv0) : );
  SB(); GM();
  MF16("a", acc2[0][2], wd[6][2], H[6]);
  asm volatile("v_mul_f32 %0, %3, %3\n\tv_lshl_or_b32 %1, %5, 16, %4\n\tv_lshl_or_b32 %2, %7, 16, %6" : "=&v"(rsq0), "=&v"(yp3_0), "=&v"(yp3_1) : "v"(cv0), "v"(ey0), "v"(ey1), "v"(ey2), "v"(ey3));
  SB(); GM();
  WAITL(2);
  MF16("v", acc2[0][3], xf[2], H[6]);
  { const unsigned so_ = sHl + 0 * H_LAYER + 1839104u; BLOAD4(H[4], v_l16, so_, 0); }
  SB(); GM();
  MF16("a", acc2[0][0], wd[7][0], H[7]);
  { const unsigned so_ = sGl + 0 * GR_KIND + 114688u; GLOADG(gD, 240, 243, so_); }
  asm volatile("v_lshlrev_b32 %0, 16, %0" : "+v"(er0) : );
  SB(); GM();
  MF16("a", acc2[0][1], wd[7][1], H[7]);
  DSR128(xf[5], wd_rd, 9216);
  asm volatile("v_lshlrev_b32 %0, 16, %0\n\tv_lshlrev_b32 %1, 16, %1" : "+v"(ey0), "+v"(er1) : );
  SB(); GM();
  MF16("a", acc2[0][2], wd[7][2], H[7]);
  { const unsigned so_ = sHl + 0 * H_LAYER + 1839104u; BLOAD4(H[5], v_l16, so_, 1024); }
  asm volatile("v_lshlrev_b32 %0, 16, %0\n\tv_lshlrev_b32 %1, 16, %1" : "+v"(ey1), "+v"(er2) : );
  SB(); GM();
  WAITL(2);
  MF16("v", acc2[0][3], xf[3], H[7]);
  asm volatile("v_lshlrev_b32 %0, 16, %0\n\tv_lshlrev_b32 %1, 16, %1" : "+v"(ey2), "+v"(er3) : );
  SB(); GM();
  MF16("a", acc2[0][0], wd[8][0], H[8]);
  asm volatile("v_lshlrev_b32 %0, 16, %0\n\tv_add_f32 %1, %3, %4\n\tv_mul_f32 %2, %3, %3" : "+v"(ey3), "=&v"(ea), "=&v"(eb) : "v"(er0), "v"(er1));
  SB(); GM();
  MF16("a", acc2[0][1], wd[8][1], H[8]);
  DSR128(xf[6], wd_rd, 10240);
  asm volatile("v_add_f32 %0, %2, %3\n\tv_add_f32 %1, %1, %4" : "=&v"(ec), "+v"(ea) : "v"(ey0), "v"(ey1), "v"(er2));
  SB(); GM();
  MF16("a", acc2[0][2], wd[8][2], H[8]);
  { const unsigned so_ = sHl + 0 * H_LAYER + 1839104u; BLOAD4(H[6], v_l16, so_, 2048); }
  asm volatile("v_fma_f32 %0, %2, %2, %0\n\tv_add_f32 %1, %1, %3" : "+v"(eb), "+v"(ec) : "v"(er1), "v"(ey2));
  SB(); GM();
  WAITL(2);
  MF16("v", acc2[0][3], xf[4], H[8]);
  asm volatile("v_add_f32 %0, %0, %2\n\tv_fma_f32 %1, %3, %3, %1" : "+v"(ea), "+v"(eb) : "v"(er3), "v"(er2));
  SB(); GM();
  MF16("a", acc2[0][0], wd[9][0], H[9]);
  asm volatile("v_add_f32 %0, %0, %3\n\tv_fma_f32 %1, %4, %4, %1\n\tv_add_f32_dpp %2, %5, %5 row_ror:8 row_mask:0xf bank_mask:0xf" : "+v"(ec), "+v"(eb), "=&v"(et) : "v"(ey3), "v"(er3), "v"(ea));
  SB(); GM();
  MF16("a", acc2[0][1], wd[9][1], H[9]);
  DSR128(xf[7], wd_rd, 11264);
  { const unsigned so_ = sHl + 0 * H_LAYER + 1839104u; BLOAD4(H[7], v_l16, so_, 3072); }
  asm volatile("v_add_f32_dpp %0, %0, %0 row_ror:8 row_mask:0xf bank_mask:0xf" : "+v"(ec) : );
  SB(); GM();
  MF16("a", acc2[0][2], wd[9][2], H[9]);
  WAITV(9);
  { const unsigned so_ = sHf + 688128u; BSTORE2(tagv, v_one, so_); }
  SB(); GM();
  WAITL(2);
  MF16("v", acc2[0][3], xf[5], H[9]);
  asm volatile("v_add_f32_dpp %0, %2, %2 row_ror:8 row_mask:0xf bank_mask:0xc\n\ts_nop 1\n\tv_add_f32_dpp %1, %0, %0 row_half_mirror row_mask:0xf bank_mask:0xf" : "+v"(et), "=&v"(eu) : "v"(eb));
  SB(); GM();
  MF16("a", acc2[0][0], wd[10][0], H[10]);
  asm volatile("v_add_f32_dpp %0, %1, %1 row_half_mirror row_mask:0xf bank_mask:0x2\n\ts_nop 1\n\tv_add_f32_dpp %0, %0, %0 quad_perm:[1,0,3,2] row_mask:0xf bank_mask:0xf\n\ts_nop 1\n\tv_add_f32_dpp %0, %0, %0 quad_perm:[2,3,0,1] row_mask:0xf bank_mask:0xf" : "+v"(eu) : "v"(ec));
  SB(); GM();
  MF16("a", acc2[0][1], wd[10][1], H[10]);
  DSR128(xf[0], wd_rd, 12288);
  { const unsigned so_ = sHl + 0 * H_LAYER + 1843200u; BLOAD4(H[8], v_l16, so_, 0); }
  SB(); GM();
  MF16("a", acc2[0][2], wd[10][2], H[10]);
  { gp[0] = f2u(eu); const unsigned so_ = sGs + 0 * GR_KIND + 344064u; BSTORE2(gp, v_gst, so_); }
  SB(); GM();
  WAITL(2);
  MF16("v", acc2[0][3], xf[6], H[10]);
  SB(); GM();
  MF16("a", acc2[0][0], wd[11][0], H[11]);
  SB(); GM();
  MF16("a", acc2[0][1], wd[11][1], H[11]);
  DSR128(xf[1], wd_rd, 13312);
  { const unsigned so_ = sHl + 0 * H_LAYER + 1843200u; BLOAD4(H[9], v_l16, so_, 1024); }
  SB(); GM();
  MF16("a", acc2[0][2], wd[11][2], H[11]);
  SB(); GM();
  WAITL(2);
  MF16("v", acc2[0][3], xf[7], H[11]);
  SBAR();
  DSRTR(xf[2], xadr[0], 0, 256);
  SB(); GM();
  MF16("a", acc2[0][0], wd[12][0], H[12]);
  SB(); GM();
  MF16("a", acc2[0][1], wd[12][1], H[12]);
  DSRTR(xf[3], xadr[0], 4096, 4352);
  { const unsigned so_ = sHl + 0 * H_LAYER + 1843200u; BLOAD4(H[10], v_l16, so_, 2048); }
  SB(); GM();
  MF16("a", acc2[0][2], wd[12][2], H[12]);
  SB(); GM();
  WAITL(5);
  MF16("v", acc2[0][3], xf[0], H[12]);
  DSRTR(xf[4], xadr[0], 8192, 8448);
  SB(); GM();
  MF16("a", acc2[0][0], wd[13][0], H[13]);
  SB(); GM();
  MF16("a", acc2[0][1], wd[13][1], H[13]);
  DSRTR(xf[5], xadr[0], 12288, 12544);
  { const unsigned so_ = sHl + 0 * H_LAYER + 1843200u; BLOAD4(H[11], v_l16, so_, 3072); }
  SB(); GM();
  MF16("a", acc2[0][2], wd[13][2], H[13]);
  SB(); GM();
  WAITL(8);
  MF16("v", acc2[0][3], xf[1], H[13]);
  DSRTR(xf[6], xadr[0], 16384, 16640);
  SB(); GM();
  WAITL(8);
  MF32Z("v", acc[1], wg[0], xf[2]);
  DSRTR(xf[7], xadr[0], 20480, 20736);
  asm volatile("v_add_u32 %0, %1, %2" : "=v"(xadr[1]) : "s"(sXg[1] + 28672u), "v"(xl0));
  MF16Z("v", sacc, sel, xf[2]);
  { const unsigned so_ = sHl + 0 * H_LAYER + 1847296u; BLOAD4(H[12], v_l16, so_, 0); }
  SB(); GM();
  WAITL(8);
  MF32("v", acc[1], wg[1], xf[3]);
  DSRTR(xf[0], xadr[0], 24576, 24832);
  DSW128(red_wr, acc2[0][0], 0);
  MF16("v", sacc, sel, xf[3]);
  { const unsigned so_ = sHl + 0 * H_LAYER + 1847296u; BLOAD4(H[13], v_l16, so_, 1024); }
  DSW128(rw1, acc2[0][1], 0);
  SB(); GM();
  WAITL(10);
  MF32("v", acc[1], wg[2], xf[4]);
  DSRTR(xf[1], xadr[1], 0, 256);
  MF16("v", sacc, sel, xf[4]);
  DSW128(rw2, acc2[0][2], 0);
  SB(); GM();
  WAITL(11);
  MF32("v", acc[1], wg[3], xf[5]);
  DSRTR(xf[2], xadr[1], 4096, 4352);
  MF16("v", sacc, sel, xf[5]);
  DSW128(rw3, acc2[0][3], 0);
  SB(); GM();
  WAITL(12);
  MF32("v", acc[1], wg[4], xf[6]);
  DSRTR(xf[3], xadr[1], 8192, 8448);
  MF16("v", sacc, sel, xf[6]);
  SB(); GM();
  WAITL(12);
  MF32("v", acc[1], wg[5], xf[7]);
  DSRTR(xf[4], xadr[1], 12288, 12544);
  MF16("v", sacc, sel, xf[7]);
  SB(); GM();
  WAITL(12);
  MF32("a", acc[1], wg[6], xf[0]);
  DSRTR(xf[5], xadr[1], 16384, 16640);
  MF16("v", sacc, sel, xf[0]);
  WAITL(6);
  SBAR();
  DSR128(pr[0], red_rd, 0);
  DSR128(pr[1], red_rd, 4096);
  SB(); GM();
  MF32("a", acc[1], wg[7], xf[1]);
  DSRTR(xf[6], xadr[1], 20480, 20736);
  asm volatile("v_add_u32 %0, %1, %2" : "=v"(xadr[0]) : "s"(sXg[2] + 28672u), "v"(xl0));
  DSR128(pr[2], red_rd, 8192);
  DSR128(pr[3], red_rd, 12288);
  SB(); GM();
  MF32("a", acc[1], wg[8], xf[2]);
  DSRTR(xf[7], xadr[1], 24576, 24832);
  SB(); GM();
  WAITL(12);
  MF32("a", acc[1], wg[9], xf[3]);
  DSRTR(xf[0], xadr[0], 0, 256);
  { asm volatile("" :: "v"(sacc)); float s0_ = sacc[0]; DSW32(st_wr, s0_, 512); }
  SB(); GM();
  WAITL(13);
  MF32("a", acc[1], wg[10], xf[4]);
  DSRTR(xf[1], xadr[0], 4096, 4352);
  WAITL(7);
  asm volatile("v_add_f32 %0, %4, %5\n\tv_add_f32 %1, %6, %7\n\tv_add_f32 %2, %8, %9\n\tv_add_f32 %3, %10, %11" : "=&v"(fo0), "=&v"(fo1), "=&v"(fo2), "=&v"(fo3) : "v"(pr[0][0]), "v"(pr[1][0]), "v"(pr[0][1]), "v"(pr[1][1]), "v"(pr[0][2]), "v"(pr[1][2]), "v"(pr[0][3]), "v"(pr[1][3]));
  SB(); GM();
  MF32("a", acc[1], wg[11], xf[5]);
  DSRTR(xf[2], xadr[0], 8192, 8448);
  asm volatile("v_add_f32 %0, %0, %4\n\tv_add_f32 %1, %1, %5\n\tv_add_f32 %2, %2, %6\n\tv_add_f32 %3, %3, %7\n\tv_add_f32 %0, %0, %8" : "+v"(fo0), "+v"(fo1), "+v"(fo2), "+v"(fo3) : "v"(pr[2][0]), "v"(pr[2][1]), "v"(pr[2][2]), "v"(pr[2][3]), "v"(pr[3][0]));
  SB(); GM();
  MF32("a", acc[1], wg[12], xf[6]);
  DSRTR(xf[3], xadr[0], 12288, 12544);
  asm volatile("v_add_f32 %0, %0, %4\n\tv_add_f32 %1, %1, %5\n\tv_add_f32 %2, %2, %6\n\tv_fma_f32 %3, %3, %7, %8\n\tv_fma_f32 %0, %0, %7, %9" : "+v"(fo1), "+v"(fo2), "+v"(fo3), "+v"(fo0) : "v"(pr[3][1]), "v"(pr[3][2]), "v"(pr[3][3]), "v"(rsq0), "v"(rf0), "v"(rf1));
  SB(); GM();
  WAITL(9);
  MF32("a", acc[1], wg[13], xf[7]);
  DSRTR(xf[4], xadr[0], 16384, 16640);
  asm volatile("v_fma_f32 %0, %0, %5, %6\n\tv_fma_f32 %1, %1, %5, %7\n\tv_cvt_pk_bf16_f32 %2, %8, %9\n\tv_cvt_pk_bf16_f32 %3, %0, %1\n\tv_lshlrev_b32 %4, 16, %2" : "+v"(fo2), "+v"(fo3), "=&v"(fp0), "=&v"(fp1), "=&v"(x20_0) : "v"(rsq0), "v"(rf2), "v"(rf3), "v"(fo0), "v"(fo1));
  SB(); GM();
  WAITL(9);
  MF32("a", acc[1], wg[14], xf[0]);
  DSRTR(xf[5], xadr[0], 20480, 20736);
  asm volatile("v_add_u32 %0, %1, %2" : "=v"(xadr[1]) : "s"(sXg[3] + 28672u), "v"(xl0));
  WAITL(10);
  SBAR();
  asm volatile("v_and_b32 %0, 0xffff0000, %2\n\tv_lshlrev_b32 %1, 16, %3" : "=&v"(x20_1), "=&v"(x20_2) : "v"(fp0), "v"(fp1));
  SB(); GM();
  WAITL(8);
  MF32("a", acc[1], wg[15], xf[1]);
  DSRTR(xf[6], xadr[0], 24576, 24832);
  DSR128(stv, st_rd, 512);
  asm volatile("v_and_b32 %0, 0xffff0000, %4\n\tv_add_f32 %1, %5, %6\n\tv_mul_f32 %2, %5, %5\n\tv_mul_f32 %3, %5, %7" : "=&v"(x20_3), "=&v"(fa), "=&v"(fb), "=&v"(fc) : "v"(fp1), "v"(x20_0), "v"(x20_1), "v"(yf0_0));
  SB(); GM();
  WAITL(9);
  MF32("a", acc[1], wg[16], xf[2]);
  DSRTR(xf[7], xadr[1], 0, 256);
  asm volatile("v_add_f32 %0, %0, %3\n\tv_fma_f32 %1, %4, %4, %1\n\tv_fma_f32 %2, %4, %5, %2\n\tv_add_f32 %0, %0, %6\n\tv_fma_f32 %2, %3, %7, %2" : "+v"(fa), "+v"(fb), "+v"(fc) : "v"(x20_2), "v"(x20_1), "v"(yf0_1), "v"(x20_3), "v"(yf0_2));
  SB(); GM();
  WAITL(9);
  MF32("a", acc[1], wg[17], xf[3]);
  DSRTR(xf[0], xadr[1], 4096, 4352);
  asm volatile("v_fma_f32 %0, %3, %3, %0\n\tv_fma_f32 %1, %4, %5, %1\n\tv_fma_f32 %0, %4, %4, %0\n\tv_add_f32_dpp %2, %6, %6 row_ror:8 row_mask:0xf bank_mask:0xf\n\tv_add_f32_dpp %1, %1, %1 row_ror:8 row_mask:0xf bank_mask:0xf" : "+v"(fb), "+v"(fc), "=&v"(ft0) : "v"(x20_2), "v"(x20_3), "v"(yf0_3), "v"(fa));
  SB(); GM();
  WAITL(9);
  MF32("a", acc[1], wg[18], xf[4]);
  DSRTR(xf[1], xadr[1], 8192, 8448);
  { const unsigned so_ = sHp + 458752u; BLOAD2(pollh, v_ph, so_); }
  WAITL(6);
  asm volatile("v_add_f32_dpp %0, %2, %2 row_ror:8 row_mask:0xf bank_mask:0xc\n\ts_nop 1\n\tv_add_f32_dpp %1, %0, %0 row_half_mirror row_mask:0xf bank_mask:0xf" : "+v"(ft0), "=&v"(fu) : "v"(fb));
  SB(); GM();
  MF32("a", acc[1], wg[19], xf[5]);
  DSRTR(xf[2], xadr[1], 12288, 12544);
  asm volatile("v_add_f32 %0, %3, %4\n\tv_add_f32 %1, %5, %6\n\tv_add_f32 %0, %0, %1\n\tv_add_f32_dpp %2, %7, %7 row_half_mirror row_mask:0xf bank_mask:0x2\n\ts_nop 1\n\tv_add_f32_dpp %2, %2, %2 quad_perm:[1,0,3,2] row_mask:0xf bank_mask:0xf" : "=&v"(mt0), "=&v"(mt1), "+v"(fu) : "v"(stv[0]), "v"(stv[1]), "v"(stv[2]), "v"(stv[3]), "v"(fc));
  SB(); GM();
  MF32("a", acc[1], wg[20], xf[6]);
  DSRTR(xf[3], xadr[1], 16384, 16640);
  asm volatile("v_mul_f32 %0, 0xbb124925, %0\n\tv_cvt_pk_bf16_f32 %1, %0, %0" : "+v"(mt0), "=&v"(mtp) : );
  bext[0] = mtp;
  asm volatile("v_add_f32_dpp %0, %0, %0 quad_perm:[2,3,0,1] row_mask:0xf bank_mask:0xf" : "+v"(fu) : );
  { gp[0] = f2u(fu); const unsigned so_ = sGs + 1 * GR_KIND + 0u; BSTORE2(gp, v_gst, so_); }
  SB(); GM();
  WAITL(8);
  MF32("a", acc[1], wg[21], xf[7]);
  DSRTR(xf[4], xadr[1], 20480, 20736);
  asm volatile("v_add_u32 %0, %1, %2" : "=v"(xadr[0]) : "s"(sXg[0] + 114688u), "v"(xl1));
  WAITV(13);
  { const unsigned so_ = sGl + 0 * GR_KIND + 114688u; CHECKG(gD, 240, 241, 243, so_); }
  SB(); GM();
  WAITL(8);
  MF32("a", acc[1], wg[22], xf[0]);
  DSRTR(xf[5], xadr[1], 24576, 24832);
  asm volatile("v_add_f32_dpp %0, %2, %2 row_ror:2 row_mask:0xf bank_mask:0xf\n\tv_add_f32_dpp %1, %3, %3 row_ror:2 row_mask:0xf bank_mask:0xf\n\ts_nop 0\n\tv_add_f32_dpp %0, %0, %0 row_ror:4 row_mask:0xf bank_mask:0xf" : "=&v"(cp1), "=&v"(cq1) : "v"(gD[0]), "v"(gD[2]));
  SB(); GM();
  WAITL(8);
  MF32("a", acc[1], wg[23], xf[1]);
  DSR128(xf[6], wd_rd, 0);
  asm volatile("v_add_f32_dpp %0, %0, %0 row_ror:4 row_mask:0xf bank_mask:0xf\n\tv_add_f32_dpp %1, %1, %1 row_ror:8 row_mask:0xf bank_mask:0xf\n\ts_nop 0\n\tv_add_f32_dpp %0, %0, %0 row_ror:8 row_mask:0xf bank_mask:0xf\n\tv_mul_f32_dpp %2, %1, %3 quad_perm:[0,0,2,2] row_mask:0xf bank_mask:0xf" : "+v"(cq1), "+v"(cp1), "=&v"(cm1) : "v"(vinvD));
  SB(); GM();
  WAITL(7);
  MF32("a", acc[1], wg[24], xf[2]);
  DSR128(xf[7], wd_rd, 1024);
  asm volatile("v_mul_f32_dpp %0, %2, %3 quad_perm:[1,1,3,3] row_mask:0xf bank_mask:0xf\n\tv_mul_f32_dpp %1, %4, %3 quad_perm:[0,0,2,2] row_mask:0xf bank_mask:0xf\n\tv_fma_f32 %0, -%5, %5, %0\n\tv_max_f32 %0, 0, %0" : "=&v"(cv1), "=&v"(sy1) : "v"(cp1), "v"(vinvD), "v"(cq1), "v"(cm1));
  SB(); GM();
  WAITL(6);
  MF32("a", acc[1], wg[25], xf[3]);
  DSR128(xf[0], wd_rd, 2048);
  asm volatile("v_cvt_pk_bf16_f32 %0, %1, %1\n\tv_lshlrev_b32 %1, 16, %0\n\tv_add_f32 %1, %2, %1\n\tv_cvt_pk_bf16_f32 %0, %1, %1" : "=&v"(ct1), "+v"(cv1) : "s"(sEps));
  SB(); GM();
  WAITL(5);
  MF32("a", acc[1], wg[26], xf[4]);
  DSR128(xf[1], wd_rd, 3072);
  asm volatile("v_lshlrev_b32 %0, 16, %1\n\tv_sqrt_f32 %0, %0\n\ts_nop 0\n\tv_cvt_pk_bf16_f32 %1, %0, %0" : "=&v"(cv1), "+v"(ct1) : );
  SB(); GM();
  WAITL(4);
  MF32("a", acc[1], wg[27], xf[5]);
  DSR128(xf[2], wd_rd, 4096);
  asm volatile("v_lshlrev_b32 %0, 16, %2\n\tv_rcp_f32 %0, %0\n\ts_nop 0\n\tv_mul_f32 %1, %0, %0" : "=&v"(cv1), "=&v"(rsq1) : "v"(ct1));
  SB(); GM();
  MF32("v", acc[1], aext, bext);
  DSR128(xf[3], wd_rd, 5120);
  { const unsigned so_ = sGl + 0 * GR_KIND + 229376u; GLOADG(gA, 244, 247, so_); }
  SB(); STUB(12, ".Lwret12"); GM();
  MF16Z("a", acc2[1][0], wd[0][0], H[0]);
  asm volatile("v_lshlrev_b32 %0, 16, %5\n\tv_and_b32 %1, 0xffff0000, %5\n\tv_lshlrev_b32 %2, 16, %6\n\tv_and_b32 %3, 0xffff0000, %6\n\tv_lshlrev_b32 %4, 16, %7" : "=&v"(rf0), "=&v"(rf1), "=&v"(rf2), "=&v"(rf3), "=&v"(yf1_0) : "v"(rp1_0), "v"(rp1_1), "v"(yp1_0));
  SB(); GM();
  MF16Z("a", acc2[1][1], wd[0][1], H[0]);
  WAITV(2);
  { const unsigned so_ = sHp + 458752u; CHECKF(pollh, v_ph, so_); }
  asm volatile("v_and_b32 %0, 0xffff0000, %2\n\tv_lshlrev_b32 %1, 16, %3" : "=&v"(yf1_1), "=&v"(yf1_2) : "v"(yp1_0), "v"(yp1_1));
  SB(); GM();
  MF16Z("a", acc2[1][2], wd[0][2], H[0]);
  asm volatile("v_and_b32 %0, 0xffff0000, %4\n\tv_xor_b32 %1, 64, %5\n\tv_xor_b32 %2, 128, %5\n\tv_xor_b32 %3, 192, %5" : "=&v"(yf1_3), "=&v"(rw1), "=&v"(rw2), "=&v"(rw3) : "v"(yp1_1), "v"(red_wr));
  SB(); GM();
  WAITL(5);
  MF16Z("v", acc2[1][3], xf[6], H[0]);
  { const unsigned so_ = sHl + 0 * H_LAYER + 3670016u; BLOAD4(H[0], v_l16, so_, 0); }
  asm volatile("v_max_f32 %0, 0, %2\n\tv_max_f32 %1, 0, %3" : "=&v"(ep0), "=&v"(ep1) : "v"(acc[1][0]), "v"(acc[1][1]));
  SB(); GM();
  MF16("a", acc2[1][0], wd[1][0], H[1]);
  asm volatile("v_mul_f32 %0, %0, %3\n\tv_mul_f32 %1, %1, %4\n\tv_cvt_pk_bf16_f32 %2, %0, %1\n\tv_max_f32 %0, 0, %5\n\tv_max_f32 %1, 0, %6" : "+v"(ep0), "+v"(ep1), "=&v"(hp0) : "v"(acc[1][8]), "v"(acc[1][9]), "v"(acc[1][2]), "v"(acc[1][3]));
  SB(); GM();
  MF16("a", acc2[1][1], wd[1][1], H[1]);
  asm volatile("v_mul_f32 %0, %0, %3\n\tv_mul_f32 %1, %1, %4\n\tv_cvt_pk_bf16_f32 %2, %0, %1\n\tv_max_f32 %0, 0, %5\n\tv_max_f32 %1, 0, %6" : "+v"(ep0), "+v"(ep1), "=&v"(hp1) : "v"(acc[1][10]), "v"(acc[1][11]), "v"(acc[1][4]), "v"(acc[1][5]));
  SB(); GM();
  MF16("a", acc2[1][2], wd[1][2], H[1]);
  asm volatile("v_mul_f32 %0, %0, %3\n\tv_mul_f32 %1, %1, %4\n\tv_cvt_pk_bf16_f32 %2, %0, %1\n\tv_max_f32 %0, 0, %5\n\tv_max_f32 %1, 0, %6" : "+v"(ep0), "+v"(ep1), "=&v"(hp2) : "v"(acc[1][12]), "v"(acc[1][13]), "v"(acc[1][6]), "v"(acc[1][7]));
  SB(); GM();
  WAITL(4);
  MF16("v", acc2[1][3], xf[7], H[1]);
  { const unsigned so_ = sHl + 0 * H_LAYER + 3670016u; BLOAD4(H[1], v_l16, so_, 1024); }
  asm volatile("v_mul_f32 %0, %0, %3\n\tv_mul_f32 %1, %1, %4\n\tv_cvt_pk_bf16_f32 %2, %0, %1" : "+v"(ep0), "+v"(ep1), "=&v"(hp3) : "v"(acc[1][14]), "v"(acc[1][15]));
  SB(); GM();
  MF16("a", acc2[1][0], wd[2][0], H[2]);
  { hfr = v4u{hp0, hp1, hp2, hp3}; const unsigned so_ = sHst + 0 * H_LAYER + 6422528u; BSTORE4(hfr, v_hst, so_); }
  SB(); GM();
  MF16("a", acc2[1][1], wd[2][1], H[2]);
  SB(); GM();
  MF16("a", acc2[1][2], wd[2][2], H[2]);
  SB(); GM();
  WAITL(3);
  MF16("v", acc2[1][3], xf[0], H[2]);
  { const unsigned so_ = sHl + 0 * H_LAYER + 3670016u; BLOAD4(H[2], v_l16, so_, 2048); }
  SB(); GM();
  MF16("a", acc2[1][0], wd[3][0], H[3]);
  SB(); GM();
  MF16("a", acc2[1][1], wd[3][1], H[3]);
  SB(); GM();
  MF16("a", acc2[1][2], wd[3][2], H[3]);
  SB(); GM();
  WAITL(2);
  MF16("v", acc2[1][3], xf[1], H[3]);
  { const unsigned so_ = sHl + 0 * H_LAYER + 3670016u; BLOAD4(H[3], v_l16, so_, 3072); }
  SB(); GM();
  MF16("a", acc2[1][0], wd[4][0], H[4]);
  SB(); GM();
  MF16("a", acc2[1][1], wd[4][1], H[4]);
  DSR128(xf[4], wd_rd, 6144);
  SB(); GM();
  MF16("a", acc2[1][2], wd[4][2], H[4]);
  SB(); GM();
  WAITL(2);
  MF16("v", acc2[1][3], xf[2], H[4]);
  { const unsigned so_ = sHl + 0 * H_LAYER + 3674112u; BLOAD4(H[4], v_l16, so_, 0); }
  SB(); GM();
  MF16("a", acc2[1][0], wd[5][0], H[5]);
  WAITV(6);
  { const unsigned so_ = sGl + 0 * GR_KIND + 229376u; CHECKG(gA, 244, 245, 247, so_); }
  SB(); GM();
  MF16("a", acc2[1][1], wd[5][1], H[5]);
  DSR128(xf[5], wd_rd, 7168);
  asm volatile("v_add_f32_dpp %0, %2, %2 row_ror:2 row_mask:0xf bank_mask:0xf\n\tv_add_f32_dpp %1, %3, %3 row_ror:2 row_mask:0xf bank_mask:0xf" : "=&v"(cp0), "=&v"(cq0) : "v"(gA[0]), "v"(gA[2]));
  SB(); GM();
  MF16("a", acc2[1][2], wd[5][2], H[5]);
  asm volatile("v_add_f32_dpp %0, %0, %0 row_ror:4 row_mask:0xf bank_mask:0xf\n\tv_add_f32_dpp %1, %1, %1 row_ror:4 row_mask:0xf bank_mask:0xf\n\ts_nop 0\n\tv_add_f32_dpp %0, %0, %0 row_ror:8 row_mask:0xf bank_mask:0xf" : "+v"(cp0), "+v"(cq0) : );
  SB(); GM();
  WAITL(2);
  MF16("v", acc2[1][3], xf[3], H[5]);
  { const unsigned so_ = sHl + 0 * H_LAYER + 3674112u; BLOAD4(H[5], v_l16, so_, 1024); }
  asm volatile("v_add_f32_dpp %0, %0, %0 row_ror:8 row_mask:0xf bank_mask:0xf" : "+v"(cq0) : );
  SB(); GM();
  MF16("a", acc2[1][0], wd[6][0], H[6]);
  asm volatile("v_mul_f32_dpp %0, %3, %4 quad_perm:[0,0,2,2] row_mask:0xf bank_mask:0xf\n\tv_mul_f32_dpp %1, %3, %4 quad_perm:[1,1,3,3] row_mask:0xf bank_mask:0xf\n\tv_mul_f32_dpp %2, %5, %4 quad_perm:[0,0,2,2] row_mask:0xf bank_mask:0xf" : "=&v"(cm0), "=&v"(cv0), "=&v"(sy2) : "v"(cp0), "v"(vinvD), "v"(cq0));
  SB(); GM();
  MF16("a", acc2[1][1], wd[6][1], H[6]);
  DSR128(xf[6], wd_rd, 8192);
  asm volatile("v_fma_f32 %0, -%1, %1, %0\n\tv_max_f32 %0, 0, %0" : "+v"(cv0) : "v"(cm0));
  SB(); GM();
  MF16("a", acc2[1][2], wd[6][2], H[6]);
  asm volatile("v_cvt_pk_bf16_f32 %0, %1, %1\n\tv_lshlrev_b32 %1, 16, %0\n\tv_add_f32 %1, %2, %1" : "=&v"(ct0), "+v"(cv0) : "s"(sEps));
  SB(); GM();
  WAITL(2);
  MF16("v", acc2[1][3], xf[4], H[6]);
  { const unsigned so_ = sHl + 0 * H_LAYER + 3674112u; BLOAD4(H[6], v_l16, so_, 2048); }
  asm volatile("v_cvt_pk_bf16_f32 %0, %1, %1" : "=v"(ct0) : "v"(cv0));
  SB(); GM();
  MF16("a", acc2[1][0], wd[7][0], H[7]);
  asm volatile("v_lshlrev_b32 %0, 16, %1\n\tv_sqrt_f32 %0, %0" : "=&v"(cv0) : "v"(ct0));
  SB(); GM();
  MF16("a", acc2[1][1], wd[7][1], H[7]);
  DSR128(xf[7], wd_rd, 9216);
  asm volatile("v_cvt_pk_bf16_f32 %0, %1, %1\n\tv_lshlrev_b32 %1, 16, %0" : "=&v"(ct0), "+v"(cv0) : );
  SB(); GM();
  MF16("a", acc2[1][2], wd[7][2], H[7]);
  asm volatile("v_rcp_f32 %0, %0\n\ts_nop 0\n\tv_mul_f32 %1, %0, %0" : "+v"(cv0), "=&v"(rsq2) : );
  SB(); GM();
  WAITL(2);
  MF16("v", acc2[1][3], xf[5], H[7]);
  { const unsigned so_ = sHl + 0 * H_LAYER + 3674112u; BLOAD4(H[7], v_l16, so_, 3072); }
  SB(); GM();
  MF16("a", acc2[1][0], wd[8][0], H[8]);
  SB(); GM();
  MF16("a", acc2[1][1], wd[8][1], H[8]);
  DSR128(xf[0], wd_rd, 10240);
  SB(); GM();
  MF16("a", acc2[1][2], wd[8][2], H[8]);
  SB(); GM();
  WAITL(2);
  MF16("v", acc2[1][3], xf[6], H[8]);
  { const unsigned so_ = sHl + 0 * H_LAYER + 3678208u; BLOAD4(H[8], v_l16, so_, 0); }
  SB(); GM();
  MF16("a", acc2[1][0], wd[9][0], H[9]);
  { const unsigned so_ = sGl + 0 * GR_KIND + 344064u; GLOADG(gD, 240, 243, so_); }
  SB(); GM();
  MF16("a", acc2[1][1], wd[9][1], H[9]);
  DSR128(xf[1], wd_rd, 11264);
  SB(); GM();
  MF16("a", acc2[1][2], wd[9][2], H[9]);
  SB(); GM();
  WAITL(2);
  MF16("v", acc2[1][3], xf[7], H[9]);
  SB(); GM();
  MF16("a", acc2[1][0], wd[10][0], H[10]);
  SB(); GM();
  MF16("a", acc2[1][1], wd[10][1], H[10]);
  DSR128(xf[2], wd_rd, 12288);
  SB(); GM();
  MF16("a", acc2[1][2], wd[10][2], H[10]);
  SB(); GM();
  WAITL(2);
  MF16("v", acc2[1][3], xf[0], H[10]);
  SB(); GM();
  MF16("a", acc2[1][0], wd[11][0], H[11]);
  SB(); GM();
  MF16("a", acc2[1][1], wd[11][1], H[11]);
  DSR128(xf[3], wd_rd, 13312);
  SB(); GM();
  MF16("a", acc2[1][2], wd[11][2], H[11]);
  SB(); GM();
  WAITL(2);
  MF16("v", acc2[1][3], xf[1], H[11]);
  SB(); GM();
  MF16("a", acc2[1][0], wd[12][0], H[12]);
  SB(); GM();
  MF16("a", acc2[1][1], wd[12][1], H[12]);
  DSR128(xf[4], wd_rd, 0);
  SB(); GM();
  MF16("a", acc2[1][2], wd[12][2], H[12]);
  SB(); GM();
  WAITL(2);
  MF16("v", acc2[1][3], xf[2], H[12]);
  SB(); GM();
  MF16("a", acc2[1][0], wd[13][0], H[13]);
  SB(); GM();
  MF16("a", acc2[1][1], wd[13][1], H[13]);
  DSR128(xf[5], wd_rd, 1024);
  SB(); GM();
  MF16("a", acc2[1][2], wd[13][2], H[13]);
  SB(); GM();
  WAITL(2);
  MF16("v", acc2[1][3], xf[3], H[13]);
  SB(); GM();
  WAITV(10);
  MF16Z("a", acc2[0][0], wd[0][0], H[0]);
  SB(); GM();
  MF16Z("a", acc2[0][1], wd[0][1], H[0]);
  DSR128(xf[6], wd_rd, 2048);
  SB(); GM();
  MF16Z("a", acc2[0][2], wd[0][2], H[0]);
  SB(); GM();
  WAITL(2);
  MF16Z("v", acc2[0][3], xf[4], H[0]);
  WAITV(8);
  { const unsigned so_ = sHf + 802816u; BSTORE2(tagv, v_one, so_); }
  SB(); GM();
  MF16("a", acc2[0][0], wd[1][0], H[1]);
  { const unsigned so_ = sGl + 1 * GR_KIND + 0u; GLOADG(gB, 248, 251, so_); }
  { const unsigned so_ = sHl + 0 * H_LAYER + 3678208u; BLOAD4(H[9], v_l16, so_, 1024); }
  DSW128(red_wr, acc2[1][0], 0);
  SB(); GM();
  MF16("a", acc2[0][1], wd[1][1], H[1]);
  DSR128(xf[7], wd_rd, 3072);
  { const unsigned so_ = sHl + 0 * H_LAYER + 3678208u; BLOAD4(H[10], v_l16, so_, 2048); }
  { const unsigned so_ = sHl + 0 * H_LAYER + 3678208u; BLOAD4(H[11], v_l16, so_, 3072); }
  SB(); GM();
  MF16("a", acc2[0][2], wd[1][2], H[1]);
  DSW128(rw1, acc2[1][1], 0);
  SB(); GM();
  WAITL(4);
  MF16("v", acc2[0][3], xf[5], H[1]);
  SB(); GM();
  WAITV(12);
  MF16("a", acc2[0][0], wd[2][0], H[2]);
  DSW128(rw2, acc2[1][2], 0);
  SB(); GM();
  MF16("a", acc2[0][1], wd[2][1], H[2]);
  DSR128(xf[0], wd_rd, 4096);
  { const unsigned so_ = sHl + 0 * H_LAYER + 3682304u; BLOAD4(H[12], v_l16, so_, 0); }
  SB(); GM();
  MF16("a", acc2[0][2], wd[2][2], H[2]);
  DSW128(rw3, acc2[1][3], 0);
  SB(); GM();
  WAITL(6);
  MF16("v", acc2[0][3], xf[6], H[2]);
  SB(); GM();
  WAITV(12);
  MF16("a", acc2[0][0], wd[3][0], H[3]);
  SB(); GM();
  MF16("a", acc2[0][1], wd[3][1], H[3]);
  DSR128(xf[1], wd_rd, 5120);
  { const unsigned so_ = sHl + 0 * H_LAYER + 3682304u; BLOAD4(H[13], v_l16, so_, 1024); }
  SB(); GM();
  MF16("a", acc2[0][2], wd[3][2], H[3]);
  SB(); GM();
  WAITL(5);
  MF16("v", acc2[0][3], xf[7], H[3]);
  SB(); GM();
  WAITV(12);
  MF16("a", acc2[0][0], wd[4][0], H[4]);
  SB(); GM();
  MF16("a", acc2[0][1], wd[4][1], H[4]);
  DSR128(xf[2], wd_rd, 6144);
  SB(); GM();
  MF16("a", acc2[0][2], wd[4][2], H[4]);
  WAITL(2);
  SBAR();
  DSR128(pr[0], red_rd, 0);
  DSR128(pr[1], red_rd, 4096);
  DSR128(pr[2], red_rd, 8192);
  SB(); GM();
  MF16("v", acc2[0][3], xf[0], H[4]);
  DSR128(pr[3], red_rd, 12288);
  SB(); GM();
  WAITV(11);
  MF16("a", acc2[0][0], wd[5][0], H[5]);
  SB(); GM();
  MF16("a", acc2[0][1], wd[5][1], H[5]);
  DSR128(xf[3], wd_rd, 7168);
  SB(); GM();
  MF16("a", acc2[0][2], wd[5][2], H[5]);
  SB(); GM();
  WAITL(6);
  MF16("v", acc2[0][3], xf[1], H[5]);
  SB(); GM();
  WAITV(10);
  MF16("a", acc2[0][0], wd[6][0], H[6]);
  WAITL(1);
  asm volatile("v_add_f32 %0, %3, %4\n\tv_add_f32 %1, %5, %6\n\tv_add_f32 %2, %7, %8" : "=&v"(fo0), "=&v"(fo1), "=&v"(fo2) : "v"(pr[0][0]), "v"(pr[1][0]), "v"(pr[0][1]), "v"(pr[1][1]), "v"(pr[0][2]), "v"(pr[1][2]));
  SB(); GM();
  MF16("a", acc2[0][1], wd[6][1], H[6]);
  DSR128(xf[4], wd_rd, 8192);
  asm volatile("v_add_f32 %0, %4, %5\n\tv_add_f32 %1, %1, %6\n\tv_add_f32 %2, %2, %7\n\tv_add_f32 %3, %3, %8" : "=&v"(fo3), "+v"(fo0), "+v"(fo1), "+v"(fo2) : "v"(pr[0][3]), "v"(pr[1][3]), "v"(pr[2][0]), "v"(pr[2][1]), "v"(pr[2][2]));
  SB(); GM();
  MF16("a", acc2[0][2], wd[6][2], H[6]);
  asm volatile("v_add_f32 %0, %0, %4\n\tv_add_f32 %1, %1, %5\n\tv_add_f32 %2, %2, %6\n\tv_add_f32 %3, %3, %7\n\tv_add_f32 %0, %0, %8" : "+v"(fo3), "+v"(fo0), "+v"(fo1), "+v"(fo2) : "v"(pr[2][3]), "v"(pr[3][0]), "v"(pr[3][1]), "v"(pr[3][2]), "v"(pr[3][3]));
  SB(); GM();
  MF16("v", acc2[0][3], xf[2], H[6]);
  asm volatile("v_fma_f32 %0, %0, %5, %6\n\tv_fma_f32 %1, %1, %5, %7\n\tv_fma_f32 %2, %2, %5, %8\n\tv_fma_f32 %3, %3, %5, %9\n\tv_cvt_pk_bf16_f32 %4, %0, %1" : "+v"(fo0), "+v"(fo1), "+v"(fo2), "+v"(fo3), "=&v"(fp0) : "v"(rsq1), "v"(rf0), "v"(rf1), "v"(rf2), "v"(rf3));
  SB(); GM();
  WAITV(9);
  MF16("a", acc2[0][0], wd[7][0], H[7]);
  asm volatile("v_cvt_pk_bf16_f32 %0, %4, %5\n\tv_lshlrev_b32 %1, 16, %6\n\tv_and_b32 %2, 0xffff0000, %6\n\tv_lshlrev_b32 %3, 16, %0" : "=&v"(fp1), "=&v"(x21_0), "=&v"(x21_1), "=&v"(x21_2) : "v"(fo2), "v"(fo3), "v"(fp0));
  SB(); GM();
  MF16("a", acc2[0][1], wd[7][1], H[7]);
  DSR128(xf[5], wd_rd, 9216);
  asm volatile("v_and_b32 %0, 0xffff0000, %4\n\tv_add_f32 %1, %5, %6\n\tv_mul_f32 %2, %5, %5\n\tv_mul_f32 %3, %5, %7" : "=&v"(x21_3), "=&v"(fa), "=&v"(fb), "=&v"(fc) : "v"(fp1), "v"(x21_0), "v"(x21_1), "v"(yf1_0));
  SB(); GM();
  MF16("a", acc2[0][2], wd[7][2], H[7]);
  asm volatile("v_add_f32 %0, %0, %3\n\tv_fma_f32 %1, %4, %4, %1\n\tv_fma_f32 %2, %4, %5, %2\n\tv_add_f32 %0, %0, %6\n\tv_fma_f32 %2, %3, %7, %2" : "+v"(fa), "+v"(fb), "+v"(fc) : "v"(x21_2), "v"(x21_1), "v"(yf1_1), "v"(x21_3), "v"(yf1_2));
  SB(); GM();
  WAITL(2);
  MF16("v", acc2[0][3], xf[3], H[7]);
  asm volatile("v_fma_f32 %0, %3, %3, %0\n\tv_fma_f32 %1, %4, %5, %1\n\tv_fma_f32 %0, %4, %4, %0\n\tv_add_f32_dpp %2, %6, %6 row_ror:8 row_mask:0xf bank_mask:0xf" : "+v"(fb), "+v"(fc), "=&v"(ft0) : "v"(x21_2), "v"(x21_3), "v"(yf1_3), "v"(fa));
  SB(); GM();
  WAITV(8);
  MF16("a", acc2[0][0], wd[8][0], H[8]);
  asm volatile("v_add_f32_dpp %0, %0, %0 row_ror:8 row_mask:0xf bank_mask:0xf\n\tv_add_f32_dpp %1, %3, %3 row_ror:8 row_mask:0xf bank_mask:0xc\n\ts_nop 1\n\tv_add_f32_dpp %2, %1, %1 row_half_mirror row_mask:0xf bank_mask:0xf\n\tv_add_f32_dpp %2, %0, %0 row_half_mirror row_mask:0xf bank_mask:0x2" : "+v"(fc), "+v"(ft0), "=&v"(fu) : "v"(fb));
  SB(); GM();
  MF16("a", acc2[0][1], wd[8][1], H[8]);
  DSR128(xf[6], wd_rd, 10240);
  asm volatile("v_add_f32_dpp %0, %0, %0 quad_perm:[1,0,3,2] row_mask:0xf bank_mask:0xf\n\ts_nop 1\n\tv_add_f32_dpp %0, %0, %0 quad_perm:[2,3,0,1] row_mask:0xf bank_mask:0xf" : "+v"(fu) : );
  { gp[0] = f2u(fu); const unsigned so_ = sGs + 1 * GR_KIND + 114688u; BSTORE2(gp, v_gst, so_); }
  SB(); GM();
  MF16("a", acc2[0][2], wd[8][2], H[8]);
  asm volatile("v_lshlrev_b32 %0, 16, %4\n\tv_and_b32 %1, 0xffff0000, %4\n\tv_lshlrev_b32 %2, 16, %5\n\tv_and_b32 %3, 0xffff0000, %5" : "=&v"(rf0), "=&v"(rf1), "=&v"(rf2), "=&v"(rf3) : "v"(rp2_0), "v"(rp2_1));
  SB(); GM();
  WAITL(2);
  MF16("v", acc2[0][3], xf[4], H[8]);
  WAITV(6);
  { const unsigned so_ = sGl + 1 * GR_KIND + 0u; CHECKG(gB, 248, 249, 251, so_); }
  SB(); GM();
  asm volatile("v_add_f32_dpp %0, %2, %2 row_ror:2 row_mask:0xf bank_mask:0xf\n\tv_add_f32_dpp %1, %3, %3 row_ror:2 row_mask:0xf bank_mask:0xf\n\ts_nop 0\n\tv_add_f32_dpp %0, %0, %0 row_ror:4 row_mask:0xf bank_mask:0xf\n\tv_add_f32_dpp %1, %1, %1 row_ror:4 row_mask:0xf bank_mask:0xf\n\ts_nop 0\n\tv_add_f32_dpp %0, %0, %0 row_ror:8 row_mask:0xf bank_mask:0xf" : "=&v"(xp), "=&v"(xq) : "v"(gB[0]), "v"(gB[2]));
  asm volatile("v_add_f32_dpp %0, %0, %0 row_ror:8 row_mask:0xf bank_mask:0xf\n\ts_nop 0\n\tv_mul_f32_dpp %1, %5, %6 quad_perm:[0,0,2,2] row_mask:0xf bank_mask:0xf\n\tv_mul_f32_dpp %2, %5, %6 quad_perm:[1,1,3,3] row_mask:0xf bank_mask:0xf\n\tv_mul_f32_dpp %3, %0, %6 quad_perm:[0,0,2,2] row_mask:0xf bank_mask:0xf\n\tv_fma_f32 %2, -%1, %1, %2\n\tv_cvt_pk_bf16_f32 %4, %1, %1" : "+v"(xq), "=&v"(xm), "=&v"(xv), "=&v"(xy), "=&v"(xc) : "v"(xp), "v"(vinvD));
  SB(); GM();
  asm volatile("v_lshlrev_b32 %0, 16, %3\n\tv_max_f32 %1, 0, %1\n\tv_sub_f32 %2, %6, %0\n\tv_cvt_pk_bf16_f32 %3, %1, %1\n\tv_sub_f32 %4, %7, %0\n\tv_lshlrev_b32 %1, 16, %3\n\tv_sub_f32 %5, %8, %0" : "=&v"(xmb), "+v"(xv), "=&v"(xt0), "+v"(xc), "=&v"(xt1), "=&v"(xt2) : "v"(x20_0), "v"(x20_1), "v"(x20_2));
  asm volatile("v_add_f32 %0, %4, %0\n\tv_sub_f32 %1, %5, %6\n\tv_cvt_pk_bf16_f32 %2, %0, %0\n\tv_fma_f32 %3, -%6, %7, %3" : "+v"(xv), "=&v"(xt3), "=&v"(xc), "+v"(xy) : "s"(sEps), "v"(x20_3), "v"(xmb), "v"(sy0));
  SB(); GM();
  asm volatile("v_lshlrev_b32 %0, 16, %3\n\tv_mul_f32 %1, %1, %5\n\tv_sqrt_f32 %0, %0\n\tv_mul_f32 %2, %2, %6\n\tv_cvt_pk_bf16_f32 %3, %0, %0\n\tv_mul_f32 %4, %4, %7\n\tv_lshlrev_b32 %0, 16, %3" : "=&v"(xv), "+v"(xt0), "+v"(xt1), "+v"(xc), "+v"(xt2) : "v"(yf0_0), "v"(yf0_1), "v"(yf0_2));
  asm volatile("v_mul_f32 %0, %0, %2\n\tv_rcp_f32 %1, %1" : "+v"(xt3), "+v"(xv) : "v"(yf0_3));
  SB(); GM();
  asm volatile("s_nop 0\n\tv_fma_f32 %0, %6, %7, %0\n\tv_fma_f32 %1, %1, %6, %8\n\tv_fma_f32 %2, %2, %6, %9\n\tv_fma_f32 %3, %3, %6, %10\n\tv_fma_f32 %4, %4, %6, %11\n\tv_cvt_pk_bf16_f32 %5, %0, %0" : "+v"(xm), "+v"(xt0), "+v"(xt1), "+v"(xt2), "+v"(xt3), "=&v"(xc) : "v"(xv), "v"(xy), "v"(x20_0), "v"(x20_1), "v"(x20_2), "v"(x20_3));
  asm volatile("v_lshlrev_b32 %0, 16, %5\n\tv_cvt_pk_bf16_f32 %1, %3, %4\n\tv_cvt_pk_bf16_f32 %2, %6, %7\n\tv_lshlrev_b32 %3, 16, %1\n\tv_and_b32 %4, 0xffff0000, %1" : "=&v"(xmb), "=&v"(x3p0_0), "=&v"(x3p0_1), "+v"(xt0), "+v"(xt1) : "v"(xc), "v"(xt2), "v"(xt3));
  SB(); GM();
  WAITV(5);
  MF16("a", acc2[0][0], wd[9][0], H[9]);
  asm volatile("v_lshlrev_b32 %0, 16, %4\n\tv_and_b32 %1, 0xffff0000, %4\n\tv_sub_f32 %2, %2, %5\n\tv_sub_f32 %3, %3, %5" : "=&v"(xt2), "=&v"(xt3), "+v"(xt0), "+v"(xt1) : "v"(x3p0_1), "v"(xmb));
  SB(); GM();
  MF16("a", acc2[0][1], wd[9][1], H[9]);
  DSR128(xf[7], wd_rd, 11264);
  asm volatile("v_sub_f32 %0, %0, %4\n\tv_sub_f32 %1, %1, %4\n\tv_cvt_pk_bf16_f32 %2, %5, %6\n\tv_cvt_pk_bf16_f32 %3, %0, %1" : "+v"(xt2), "+v"(xt3), "=&v"(xo0), "=&v"(xo1) : "v"(xmb), "v"(xt0), "v"(xt1));
  SB(); GM();
  MF16("a", acc2[0][2], wd[9][2], H[9]);
  { xo = v2u{xo0, xo1}; const unsigned so_ = sX3s + 0u; BSTORE2(xo, v_xst, so_); }
  asm volatile("v_mul_f32 %0, %2, %2\n\tv_sub_f32 %1, %3, %4\n\tv_fma_f32 %0, %5, %5, %0" : "=&v"(xd0), "=&v"(dm0) : "v"(xt0), "v"(xm), "v"(xmb), "v"(xt1));
  SB(); GM();
  WAITL(2);
  MF16("v", acc2[0][3], xf[5], H[9]);
  asm volatile("v_lshlrev_b32 %0, 16, %4\n\tv_and_b32 %1, 0xffff0000, %4\n\tv_lshlrev_b32 %2, 16, %5\n\tv_and_b32 %3, 0xffff0000, %5" : "=&v"(yf0_0), "=&v"(yf0_1), "=&v"(yf0_2), "=&v"(yf0_3) : "v"(yp2_0), "v"(yp2_1));
  SB(); GM();
  WAITV(5);
  MF16("a", acc2[0][0], wd[10][0], H[10]);
  asm volatile("v_xor_b32 %0, 64, %3\n\tv_xor_b32 %1, 128, %3\n\tv_xor_b32 %2, 192, %3" : "=&v"(rw1), "=&v"(rw2), "=&v"(rw3) : "v"(red_wr));
  SBAR();
  SB(); GM();
  MF16("a", acc2[0][1], wd[10][1], H[10]);
  DSR128(xf[0], wd_rd, 12288);
  asm volatile("v_fma_f32 %0, %1, %1, %0\n\tv_fma_f32 %0, %2, %2, %0" : "+v"(xd0) : "v"(xt2), "v"(xt3));
  SB(); GM();
  MF16("a", acc2[0][2], wd[10][2], H[10]);
  asm volatile("s_nop 0\n\tv_add_f32_dpp %0, %0, %0 row_ror:8 row_mask:0xf bank_mask:0xf" : "+v"(xd0) : );
  SB(); GM();
  WAITL(2);
  MF16("v", acc2[0][3], xf[6], H[10]);
  asm volatile("v_add_f32_dpp %0, %0, %0 row_ror:4 row_mask:0xf bank_mask:0xf" : "+v"(xd0) : );
  SB(); GM();
  WAITV(4);
  MF16("a", acc2[0][0], wd[11][0], H[11]);
  asm volatile("v_add_f32_dpp %0, %0, %0 row_ror:2 row_mask:0xf bank_mask:0xf" : "+v"(xd0) : );
  SB(); GM();
  MF16("a", acc2[0][1], wd[11][1], H[11]);
  DSR128(xf[1], wd_rd, 13312);
  asm volatile("v_add_f32_dpp %0, %0, %0 row_ror:1 row_mask:0xf bank_mask:0xf" : "+v"(xd0) : );
  SB(); GM();
  MF16("a", acc2[0][2], wd[11][2], H[11]);
  { const unsigned so_ = sGl + 0 * GR_KIND + 344064u; CHECKG(gD, 240, 241, 243, so_); }
  SB(); GM();
  WAITL(2);
  MF16("v", acc2[0][3], xf[7], H[11]);
  asm volatile("v_add_f32_dpp %0, %2, %2 row_ror:2 row_mask:0xf bank_mask:0xf\n\tv_add_f32_dpp %1, %3, %3 row_ror:2 row_mask:0xf bank_mask:0xf" : "=&v"(cp1), "=&v"(cq1) : "v"(gD[0]), "v"(gD[2]));
  SB(); GM();
  WAITV(3);
  MF16("a", acc2[0][0], wd[12][0], H[12]);
  asm volatile("v_add_f32_dpp %0, %0, %0 row_ror:4 row_mask:0xf bank_mask:0xf\n\tv_add_f32_dpp %1, %1, %1 row_ror:4 row_mask:0xf bank_mask:0xf" : "+v"(cp1), "+v"(cq1) : );
  SB(); GM();
  MF16("a", acc2[0][1], wd[12][1], H[12]);
  DSR128(xf[2], wd_rd, 0);
  asm volatile("v_add_f32_dpp %0, %0, %0 row_ror:8 row_mask:0xf bank_mask:0xf\n\tv_add_f32_dpp %1, %1, %1 row_ror:8 row_mask:0xf bank_mask:0xf" : "+v"(cp1), "+v"(cq1) : );
  SB(); GM();
  MF16("a", acc2[0][2], wd[12][2], H[12]);
  asm volatile("v_mul_f32_dpp %0, %3, %4 quad_perm:[0,0,2,2] row_mask:0xf bank_mask:0xf\n\tv_mul_f32_dpp %1, %3, %4 quad_perm:[1,1,3,3] row_mask:0xf bank_mask:0xf\n\tv_mul_f32_dpp %2, %5, %4 quad_perm:[0,0,2,2] row_mask:0xf bank_mask:0xf" : "=&v"(cm1), "=&v"(cv1), "=&v"(sy3) : "v"(cp1), "v"(vinvD), "v"(cq1));
  SB(); GM();
  WAITL(2);
  MF16("v", acc2[0][3], xf[0], H[12]);
  asm volatile("v_fma_f32 %0, -%1, %1, %0\n\tv_max_f32 %0, 0, %0" : "+v"(cv1) : "v"(cm1));
  SB(); GM();
  WAITV(2);
  MF16("a", acc2[0][0], wd[13][0], H[13]);
  asm volatile("v_cvt_pk_bf16_f32 %0, %1, %1\n\tv_lshlrev_b32 %1, 16, %0" : "=&v"(ct1), "+v"(cv1) : );
  SB(); GM();
  MF16("a", acc2[0][1], wd[13][1], H[13]);
  DSR128(xf[3], wd_rd, 1024);
  asm volatile("v_add_f32 %0, %2, %0\n\tv_cvt_pk_bf16_f32 %1, %0, %0" : "+v"(cv1), "=&v"(ct1) : "s"(sEps));
  SB(); GM();
  MF16("a", acc2[0][2], wd[13][2], H[13]);
  asm volatile("v_lshlrev_b32 %0, 16, %1\n\tv_sqrt_f32 %0, %0" : "=&v"(cv1) : "v"(ct1));
  SB(); GM();
  WAITL(2);
  MF16("v", acc2[0][3], xf[1], H[13]);
  asm volatile("v_cvt_pk_bf16_f32 %0, %1, %1\n\tv_lshlrev_b32 %1, 16, %0" : "=&v"(ct1), "+v"(cv1) : );
  SB(); GM();

  asm volatile("v_rcp_f32 %0, %0\n\ts_nop 0\n\tv_mul_f32 %1, %0, %0" : "+v"(cv1), "=&v"(rsq3) : );
  SB(); GM();
  __builtin_amdgcn_s_sleep(1);
  asm volatile("s_nop 7");
  DSW128(red_wr, acc2[0][0], 0);
  SB(); GM();
  DSW128(rw1, acc2[0][1], 0);
  SB(); GM();
  DSW128(rw2, acc2[0][2], 0);
  SB(); GM();
  WAITV(0);
  { const unsigned so_ = sXf + 0u; BSTORE2(tagv, v_one, so_); }
  { gp[0] = f2u(xd0); const unsigned so_ = sGs + 2 * GR_KIND + 0u; BSTORE2(gp, v_gst, so_); }
  SB(); GM();
  DSW128(rw3, acc2[0][3], 0);
  SB(); GM();
  SB(); GM();
  WAITL(0);
  SBAR();
  DSR128(pr[0], red_rd, 0);
  DSR128(pr[1], red_rd, 4096);
  DSR128(pr[2], red_rd, 8192);
  DSR128(pr[3], red_rd, 12288);
  SB(); GM();
  WAITL(0);
  asm volatile("v_add_f32 %0, %4, %5\n\tv_add_f32 %1, %6, %7\n\tv_add_f32 %2, %8, %9\n\tv_add_f32 %3, %10, %11\n\tv_add_f32 %0, %0, %12\n\tv_add_f32 %1, %1, %13\n\tv_add_f32 %2, %2, %14" : "=&v"(fo0), "=&v"(fo1), "=&v"(fo2), "=&v"(fo3) : "v"(pr[0][0]), "v"(pr[1][0]), "v"(pr[0][1]), "v"(pr[1][1]), "v"(pr[0][2]), "v"(pr[1][2]), "v"(pr[0][3]), "v"(pr[1][3]), "v"(pr[2][0]), "v"(pr[2][1]), "v"(pr[2][2]));
  asm volatile("v_add_f32 %0, %0, %3\n\tv_add_f32 %1, %1, %4\n\tv_add_f32 %2, %2, %5" : "+v"(fo3), "+v"(fo0), "+v"(fo1) : "v"(pr[2][3]), "v"(pr[3][0]), "v"(pr[3][1]));
  SB(); GM();
  asm volatile("v_add_f32 %0, %0, %5\n\tv_add_f32 %1, %1, %6\n\tv_fma_f32 %2, %2, %7, %8\n\tv_fma_f32 %3, %3, %7, %9\n\tv_fma_f32 %0, %0, %7, %10\n\tv_fma_f32 %1, %1, %7, %11\n\tv_cvt_pk_bf16_f32 %4, %2, %3" : "+v"(fo2), "+v"(fo3), "+v"(fo0), "+v"(fo1), "=&v"(fp0) : "v"(pr[3][2]), "v"(pr[3][3]), "v"(rsq2), "v"(rf0), "v"(rf1), "v"(rf2), "v"(rf3));
  asm volatile("v_cvt_pk_bf16_f32 %0, %4, %5\n\tv_lshlrev_b32 %1, 16, %6\n\tv_and_b32 %2, 0xffff0000, %6\n\tv_lshlrev_b32 %3, 16, %0" : "=&v"(fp1), "=&v"(x20_0), "=&v"(x20_1), "=&v"(x20_2) : "v"(fo2), "v"(fo3), "v"(fp0));
  SB(); GM();
  { const unsigned so_ = sHp + 688128u; BLOAD2(pollh, v_ph, so_); }
  asm volatile("v_and_b32 %0, 0xffff0000, %4\n\tv_add_f32 %1, %5, %6\n\tv_mul_f32 %2, %5, %5\n\tv_mul_f32 %3, %5, %7\n\tv_add_f32 %1, %1, %8\n\tv_fma_f32 %2, %6, %6, %2\n\tv_fma_f32 %3, %6, %9, %3" : "=&v"(x20_3), "=&v"(fa), "=&v"(fb), "=&v"(fc) : "v"(fp1), "v"(x20_0), "v"(x20_1), "v"(yf0_0), "v"(x20_2), "v"(yf0_1));
  asm volatile("v_add_f32 %0, %0, %2\n\tv_fma_f32 %1, %3, %4, %1" : "+v"(fa), "+v"(fc) : "v"(x20_3), "v"(x20_2), "v"(yf0_2));
  SB(); GM();
  asm volatile("v_fma_f32 %0, %4, %4, %0\n\tv_fma_f32 %1, %5, %6, %1\n\tv_fma_f32 %0, %5, %5, %0\n\tv_add_f32_dpp %2, %7, %7 row_ror:8 row_mask:0xf bank_mask:0xf\n\tv_add_f32_dpp %1, %1, %1 row_ror:8 row_mask:0xf bank_mask:0xf\n\tv_add_f32_dpp %2, %0, %0 row_ror:8 row_mask:0xf bank_mask:0xc\n\ts_nop 1\n\tv_add_f32_dpp %3, %2, %2 row_half_mirror row_mask:0xf bank_mask:0xf" : "+v"(fb), "+v"(fc), "=&v"(ft0), "=&v"(fu) : "v"(x20_2), "v"(x20_3), "v"(yf0_3), "v"(fa));
  asm volatile("v_add_f32_dpp %0, %1, %1 row_half_mirror row_mask:0xf bank_mask:0x2\n\ts_nop 1\n\tv_add_f32_dpp %0, %0, %0 quad_perm:[1,0,3,2] row_mask:0xf bank_mask:0xf\n\ts_nop 1\n\tv_add_f32_dpp %0, %0, %0 quad_perm:[2,3,0,1] row_mask:0xf bank_mask:0xf" : "+v"(fu) : "v"(fc));
  SB(); GM();
  { gp[0] = f2u(fu); const unsigned so_ = sGs + 1 * GR_KIND + 229376u; BSTORE2(gp, v_gst, so_); }
  SB(); GM();
  __builtin_amdgcn_s_sleep(4);
  { const unsigned so_ = sGl + 1 * GR_KIND + 114688u; GLOADG(gB, 248, 251, so_); }
  SB(); GM();
  WAITV(2);
  { const unsigned so_ = sHp + 688128u; CHECKF(pollh, v_ph, so_); }
  { const unsigned so_ = sHl + 0 * H_LAYER + 5505024u; BLOAD4(H[0], v_l16, so_, 0); }
  { const unsigned so_ = sHl + 0 * H_LAYER + 5505024u; BLOAD4(H[1], v_l16, so_, 1024); }
  SB(); GM();
  { const unsigned so_ = sHl + 0 * H_LAYER + 5505024u; BLOAD4(H[2], v_l16, so_, 2048); }
  SB(); GM();
  { const unsigned so_ = sHl + 0 * H_LAYER + 5505024u; BLOAD4(H[3], v_l16, so_, 3072); }
  SB(); GM();
  { const unsigned so_ = sHl + 0 * H_LAYER + 5509120u; BLOAD4(H[4], v_l16, so_, 0); }
  SB(); GM();
  { const unsigned so_ = sHl + 0 * H_LAYER + 5509120u; BLOAD4(H[5], v_l16, so_, 1024); }
  SB(); GM();
  WAITV(6);
  { const unsigned so_ = sGl + 1 * GR_KIND + 114688u; CHECKG(gB, 248, 249, 251, so_); }
  asm volatile("v_add_f32_dpp %0, %2, %2 row_ror:2 row_mask:0xf bank_mask:0xf\n\tv_add_f32_dpp %1, %3, %3 row_ror:2 row_mask:0xf bank_mask:0xf\n\ts_nop 0\n\tv_add_f32_dpp %0, %0, %0 row_ror:4 row_mask:0xf bank_mask:0xf\n\tv_add_f32_dpp %1, %1, %1 row_ror:4 row_mask:0xf bank_mask:0xf\n\ts_nop 0\n\tv_add_f32_dpp %0, %0, %0 row_ror:8 row_mask:0xf bank_mask:0xf" : "=&v"(xp), "=&v"(xq) : "v"(gB[0]), "v"(gB[2]));
  SB(); GM();
  { const unsigned so_ = sHl + 0 * H_LAYER + 5509120u; BLOAD4(H[6], v_l16, so_, 2048); }
  asm volatile("v_add_f32_dpp %0, %0, %0 row_ror:8 row_mask:0xf bank_mask:0xf\n\tv_mul_f32_dpp %1, %6, %7 quad_perm:[0,0,2,2] row_mask:0xf bank_mask:0xf\n\tv_mul_f32_dpp %2, %6, %7 quad_perm:[1,1,3,3] row_mask:0xf bank_mask:0xf\n\tv_mul_f32_dpp %3, %0, %7 quad_perm:[0,0,2,2] row_mask:0xf bank_mask:0xf\n\tv_fma_f32 %2, -%1, %1, %2\n\tv_cvt_pk_bf16_f32 %4, %1, %1\n\tv_lshlrev_b32 %5, 16, %4" : "+v"(xq), "=&v"(xm), "=&v"(xv), "=&v"(xy), "=&v"(xc), "=&v"(xmb) : "v"(xp), "v"(vinvD));
  asm volatile("v_max_f32 %0, 0, %0\n\tv_sub_f32 %1, %3, %4\n\tv_cvt_pk_bf16_f32 %2, %0, %0" : "+v"(xv), "=&v"(xt0), "=&v"(xc) : "v"(x21_0), "v"(xmb));
  SB(); GM();
  { const unsigned so_ = sHl + 0 * H_LAYER + 5509120u; BLOAD4(H[7], v_l16, so_, 3072); }
  asm volatile("v_sub_f32 %0, %6, %7\n\tv_lshlrev_b32 %1, 16, %4\n\tv_sub_f32 %2, %8, %7\n\tv_add_f32 %1, %9, %1\n\tv_sub_f32 %3, %10, %7\n\tv_cvt_pk_bf16_f32 %4, %1, %1\n\tv_fma_f32 %5, -%7, %11, %5" : "=&v"(xt1), "=&v"(xv), "=&v"(xt2), "=&v"(xt3), "+v"(xc), "+v"(xy) : "v"(x21_1), "v"(xmb), "v"(x21_2), "s"(sEps), "v"(x21_3), "v"(sy1));
  asm volatile("v_lshlrev_b32 %0, 16, %2\n\tv_mul_f32 %1, %1, %3" : "=&v"(xv), "+v"(xt0) : "v"(xc), "v"(yf1_0));
  SB(); GM();
  asm volatile("v_sqrt_f32 %0, %0\n\tv_mul_f32 %1, %1, %5\n\tv_cvt_pk_bf16_f32 %2, %0, %0\n\tv_mul_f32 %3, %3, %6\n\tv_lshlrev_b32 %0, 16, %2\n\tv_mul_f32 %4, %4, %7\n\tv_rcp_f32 %0, %0" : "+v"(xv), "+v"(xt1), "=&v"(xc), "+v"(xt2), "+v"(xt3) : "v"(yf1_1), "v"(yf1_2), "v"(yf1_3));
  asm volatile("s_nop 0\n\tv_fma_f32 %0, %2, %3, %0\n\tv_fma_f32 %1, %1, %2, %4" : "+v"(xm), "+v"(xt0) : "v"(xv), "v"(xy), "v"(x21_0));
  SB(); GM();
  { const unsigned so_ = sHl + 0 * H_LAYER + 5513216u; BLOAD4(H[8], v_l16, so_, 0); }
  asm volatile("v_fma_f32 %0, %0, %7, %8\n\tv_fma_f32 %1, %1, %7, %9\n\tv_fma_f32 %2, %2, %7, %10\n\tv_cvt_pk_bf16_f32 %3, %11, %11\n\tv_lshlrev_b32 %4, 16, %3\n\tv_cvt_pk_bf16_f32 %5, %12, %0\n\tv_cvt_pk_bf16_f32 %6, %1, %2" : "+v"(xt1), "+v"(xt2), "+v"(xt3), "=&v"(xc), "=&v"(xmb), "=&v"(x3p1_0), "=&v"(x3p1_1) : "v"(xv), "v"(x21_1), "v"(x21_2), "v"(x21_3), "v"(xm), "v"(xt0));
  asm volatile("v_lshlrev_b32 %0, 16, %2\n\tv_and_b32 %1, 0xffff0000, %2" : "=&v"(xt0), "=&v"(xt1) : "v"(x3p1_0));
  SB(); GM();
  { const unsigned so_ = sHl + 0 * H_LAYER + 5513216u; BLOAD4(H[9], v_l16, so_, 1024); }
  asm volatile("v_lshlrev_b32 %0, 16, %5\n\tv_and_b32 %1, 0xffff0000, %5\n\tv_sub_f32 %2, %2, %6\n\tv_sub_f32 %3, %3, %6\n\tv_sub_f32 %0, %0, %6\n\tv_sub_f32 %1, %1, %6\n\tv_cvt_pk_bf16_f32 %4, %2, %3" : "=&v"(xt2), "=&v"(xt3), "+v"(xt0), "+v"(xt1), "=&v"(xo0) : "v"(x3p1_1), "v"(xmb));
  asm volatile("v_cvt_pk_bf16_f32 %0, %1, %2" : "=v"(xo1) : "v"(xt2), "v"(xt3));
  { xo = v2u{xo0, xo1}; const unsigned so_ = sX3s + 458752u; BSTORE2(xo, v_xst, so_); }
  SB(); GM();
  asm volatile("v_mul_f32 %0, %2, %2\n\tv_sub_f32 %1, %3, %4\n\tv_fma_f32 %0, %5, %5, %0\n\tv_fma_f32 %0, %6, %6, %0\n\tv_fma_f32 %0, %7, %7, %0" : "=&v"(xd1), "=&v"(dm1) : "v"(xt0), "v"(xm), "v"(xmb), "v"(xt1), "v"(xt2), "v"(xt3));
  SB(); GM();
  asm volatile("s_nop 1\n\tv_add_f32_dpp %0, %0, %0 row_ror:8 row_mask:0xf bank_mask:0xf" : "+v"(xd1) : );
  SB(); GM();
  { const unsigned so_ = sHl + 0 * H_LAYER + 5513216u; BLOAD4(H[10], v_l16, so_, 2048); }
  SB(); GM();
  asm volatile("s_nop 0\n\tv_add_f32_dpp %0, %0, %0 row_ror:4 row_mask:0xf bank_mask:0xf" : "+v"(xd1) : );
  SB(); GM();
  asm volatile("s_nop 1\n\tv_add_f32_dpp %0, %0, %0 row_ror:2 row_mask:0xf bank_mask:0xf" : "+v"(xd1) : );
  SB(); GM();
  asm volatile("s_nop 1\n\tv_add_f32_dpp %0, %0, %0 row_ror:1 row_mask:0xf bank_mask:0xf" : "+v"(xd1) : );
  SB(); GM();
  { const unsigned so_ = sHl + 0 * H_LAYER + 5513216u; BLOAD4(H[11], v_l16, so_, 3072); }
  SB(); GM();
  { const unsigned so_ = sHl + 0 * H_LAYER + 5517312u; BLOAD4(H[12], v_l16, so_, 0); }
  SB(); GM();
  { const unsigned so_ = sHl + 0 * H_LAYER + 5517312u; BLOAD4(H[13], v_l16, so_, 1024); }
  SB(); GM();
  WAITV(14);
  MF16Z("a", acc2[1][0], wd[0][0], H[0]);
  asm volatile("v_lshlrev_b32 %0, 16, %4\n\tv_and_b32 %1, 0xffff0000, %4\n\tv_lshlrev_b32 %2, 16, %5\n\tv_and_b32 %3, 0xffff0000, %5" : "=&v"(rf0), "=&v"(rf1), "=&v"(rf2), "=&v"(rf3) : "v"(rp3_0), "v"(rp3_1));
  SB(); STUB(13, ".Lwret13"); GM();
  MF16Z("a", acc2[1][1], wd[0][1], H[0]);
  DSR128(xf[4], wd_rd, 2048);
  asm volatile("v_lshlrev_b32 %0, 16, %4\n\tv_and_b32 %1, 0xffff0000, %4\n\tv_lshlrev_b32 %2, 16, %5\n\tv_and_b32 %3, 0xffff0000, %5" : "=&v"(yf1_0), "=&v"(yf1_1), "=&v"(yf1_2), "=&v"(yf1_3) : "v"(yp3_0), "v"(yp3_1));
  SB(); GM();
  MF16Z("a", acc2[1][2], wd[0][2], H[0]);
  asm volatile("v_xor_b32 %0, 64, %3\n\tv_xor_b32 %1, 128, %3\n\tv_xor_b32 %2, 192, %3" : "=&v"(rw1), "=&v"(rw2), "=&v"(rw3) : "v"(red_wr));
  SBAR();
  SB(); GM();
  MF16Z("v", acc2[1][3], xf[2], H[0]);
  SB(); GM();
  WAITV(13);
  MF16("a", acc2[1][0], wd[1][0], H[1]);
  SB(); GM();
  MF16("a", acc2[1][1], wd[1][1], H[1]);
  DSR128(xf[5], wd_rd, 3072);
  SB(); GM();
  MF16("a", acc2[1][2], wd[1][2], H[1]);
  { const unsigned so_ = sXp + 0u; BLOAD2(pollx, v_px, so_); }
  SB(); GM();
  MF16("v", acc2[1][3], xf[3], H[1]);
  SB(); GM();
  WAITV(13);
  MF16("a", acc2[1][0], wd[2][0], H[2]);
  SB(); GM();
  MF16("a", acc2[1][1], wd[2][1], H[2]);
  DSR128(xf[6], wd_rd, 4096);
  SB(); GM();
  MF16("a", acc2[1][2], wd[2][2], H[2]);
  SB(); GM();
  WAITL(2);
  MF16("v", acc2[1][3], xf[4], H[2]);
  { const unsigned so_ = sGl + 1 * GR_KIND + 229376u; GLOADG(gB, 248, 251, so_); }
  SB(); GM();
  WAITV(13);
  MF16("a", acc2[1][0], wd[3][0], H[3]);
  SB(); GM();
  MF16("a", acc2[1][1], wd[3][1], H[3]);
  DSR128(xf[7], wd_rd, 5120);
  SB(); GM();
  MF16("a", acc2[1][2], wd[3][2], H[3]);
  SB(); GM();
  WAITL(2);
  MF16("v", acc2[1][3], xf[5], H[3]);
  SB(); GM();
  WAITV(12);
  MF16("a", acc2[1][0], wd[4][0], H[4]);
  SB(); GM();
  MF16("a", acc2[1][1], wd[4][1], H[4]);
  DSR128(xf[0], wd_rd, 6144);
  SB(); GM();
  MF16("a", acc2[1][2], wd[4][2], H[4]);
  SB(); GM();
  WAITL(2);
  MF16("v", acc2[1][3], xf[6], H[4]);
  SB(); GM();
  WAITV(11);
  MF16("a", acc2[1][0], wd[5][0], H[5]);
  SB(); GM();
  MF16("a", acc2[1][1], wd[5][1], H[5]);
  DSR128(xf[1], wd_rd, 7168);
  SB(); GM();
  MF16("a", acc2[1][2], wd[5][2], H[5]);
  SB(); GM();
  WAITL(2);
  MF16("v", acc2[1][3], xf[7], H[5]);
  SB(); GM();
  WAITV(10);
  MF16("a", acc2[1][0], wd[6][0], H[6]);
  SB(); GM();
  MF16("a", acc2[1][1], wd[6][1], H[6]);
  DSR128(xf[2], wd_rd, 8192);
  SB(); GM();
  MF16("a", acc2[1][2], wd[6][2], H[6]);
  SB(); GM();
  WAITL(2);
  MF16("v", acc2[1][3], xf[0], H[6]);
  SB(); GM();
  WAITV(9);
  MF16("a", acc2[1][0], wd[7][0], H[7]);
  SB(); GM();
  MF16("a", acc2[1][1], wd[7][1], H[7]);
  DSR128(xf[3], wd_rd, 9216);
  SB(); GM();
  MF16("a", acc2[1][2], wd[7][2], H[7]);
  SB(); GM();
  WAITL(2);
  MF16("v", acc2[1][3], xf[1], H[7]);
  SB(); GM();
  WAITV(8);
  MF16("a", acc2[1][0], wd[8][0], H[8]);
  SB(); GM();
  MF16("a", acc2[1][1], wd[8][1], H[8]);
  DSR128(xf[4], wd_rd, 10240);
  SB(); GM();
  MF16("a", acc2[1][2], wd[8][2], H[8]);
  SB(); GM();
  WAITL(2);
  MF16("v", acc2[1][3], xf[2], H[8]);
  SB(); GM();
  WAITV(7);
  MF16("a", acc2[1][0], wd[9][0], H[9]);
  WAITV(6);
  { const unsigned so_ = sXf + 114688u; BSTORE2(tagv, v_one, so_); }
  SB(); GM();
  MF16("a", acc2[1][1], wd[9][1], H[9]);
  DSR128(xf[5], wd_rd, 11264);
  { gp[0] = f2u(xd1); const unsigned so_ = sGs + 2 * GR_KIND + 114688u; BSTORE2(gp, v_gst, so_); }
  SB(); GM();
  MF16("a", acc2[1][2], wd[9][2], H[9]);
  SB(); GM();
  WAITL(2);
  MF16("v", acc2[1][3], xf[3], H[9]);
  SB(); GM();
  WAITV(7);
  MF16("a", acc2[1][0], wd[10][0], H[10]);
  SB(); GM();
  MF16("a", acc2[1][1], wd[10][1], H[10]);
  DSR128(xf[6], wd_rd, 12288);
  SB(); GM();
  MF16("a", acc2[1][2], wd[10][2], H[10]);
  SB(); GM();
  WAITL(2);
  MF16("v", acc2[1][3], xf[4], H[10]);
  SB(); GM();
  WAITV(6);
  MF16("a", acc2[1][0], wd[11][0], H[11]);
  SB(); GM();
  MF16("a", acc2[1][1], wd[11][1], H[11]);
  DSR128(xf[7], wd_rd, 13312);
  SB(); GM();
  MF16("a", acc2[1][2], wd[11][2], H[11]);
  SB(); GM();
  WAITL(2);
  MF16("v", acc2[1][3], xf[5], H[11]);
  SB(); GM();
  WAITV(5);
  MF16("a", acc2[1][0], wd[12][0], H[12]);
  SB(); GM();
  MF16("a", acc2[1][1], wd[12][1], H[12]);
  SB(); GM();
  MF16("a", acc2[1][2], wd[12][2], H[12]);
  SB(); GM();
  WAITL(1);
  MF16("v", acc2[1][3], xf[6], H[12]);
  SB(); GM();
  WAITV(4);
  MF16("a", acc2[1][0], wd[13][0], H[13]);
  WAITV(3);
  { const unsigned so_ = sXp + 0u; CHECKF(pollx, v_px, so_); }
  GLDS1(114688, sX3, 0);
  SB(); GM();
  MF16("a", acc2[1][1], wd[13][1], H[13]);
  GLDS1(0, sX3, 229376);
  SB(); GM();
  MF16("a", acc2[1][2], wd[13][2], H[13]);
  GLDS1(115712, sX3, 1024);
  SB(); GM();
  WAITL(0);
  MF16("v", acc2[1][3], xf[7], H[13]);
  WAITV(5);
  { const unsigned so_ = sGl + 1 * GR_KIND + 229376u; CHECKG(gB, 248, 249, 251, so_); }
  SB(); GM();
  GLDS1(116736, sX3, 2048);
  asm volatile("v_add_f32_dpp %0, %2, %2 row_ror:2 row_mask:0xf bank_mask:0xf\n\tv_add_f32_dpp %1, %3, %3 row_ror:2 row_mask:0xf bank_mask:0xf\n\ts_nop 0\n\tv_add_f32_dpp %0, %0, %0 row_ror:4 row_mask:0xf bank_mask:0xf\n\tv_add_f32_dpp %1, %1, %1 row_ror:4 row_mask:0xf bank_mask:0xf\n\ts_nop 0\n\tv_add_f32_dpp %0, %0, %0 row_ror:8 row_mask:0xf bank_mask:0xf" : "=&v"(xp), "=&v"(xq) : "v"(gB[0]), "v"(gB[2]));
  asm volatile("v_add_f32_dpp %0, %0, %0 row_ror:8 row_mask:0xf bank_mask:0xf\n\ts_nop 0\n\tv_mul_f32_dpp %1, %3, %4 quad_perm:[0,0,2,2] row_mask:0xf bank_mask:0xf\n\tv_mul_f32_dpp %2, %3, %4 quad_perm:[1,1,3,3] row_mask:0xf bank_mask:0xf" : "+v"(xq), "=&v"(xm), "=&v"(xv) : "v"(xp), "v"(vinvD));
  SB(); GM();
  GLDS1(1024, sX3, 230400);
  asm volatile("v_mul_f32_dpp %0, %5, %6 quad_perm:[0,0,2,2] row_mask:0xf bank_mask:0xf\n\tv_fma_f32 %1, -%7, %7, %1\n\tv_cvt_pk_bf16_f32 %2, %7, %7\n\tv_lshlrev_b32 %3, 16, %2\n\tv_max_f32 %1, 0, %1\n\tv_sub_f32 %4, %8, %3\n\tv_cvt_pk_bf16_f32 %2, %1, %1" : "=&v"(xy), "+v"(xv), "=&v"(xc), "=&v"(xmb), "=&v"(xt0) : "v"(xq), "v"(vinvD), "v"(xm), "v"(x20_0));
  asm volatile("v_sub_f32 %0, %1, %2" : "=v"(xt1) : "v"(x20_1), "v"(xmb));
  SB(); GM();
  asm volatile("s_nop 7");
  DSW128(red_wr, acc2[1][0], 0);
  asm volatile("v_lshlrev_b32 %0, 16, %3\n\tv_sub_f32 %1, %5, %6\n\tv_add_f32 %0, %7, %0\n\tv_sub_f32 %2, %8, %6\n\tv_cvt_pk_bf16_f32 %3, %0, %0\n\tv_fma_f32 %4, -%6, %9, %4\n\tv_lshlrev_b32 %0, 16, %3" : "=&v"(xv), "=&v"(xt2), "=&v"(xt3), "+v"(xc), "+v"(xy) : "v"(x20_2), "v"(xmb), "s"(sEps), "v"(x20_3), "v"(sy2));
  asm volatile("v_mul_f32 %0, %0, %1" : "+v"(xt0) : "v"(yf0_0));
  SB(); GM();
  GLDS1(117760, sX3, 3072);
  DSW128(rw1, acc2[1][1], 0);
  asm volatile("v_sqrt_f32 %0, %0\n\tv_mul_f32 %1, %1, %4\n\tv_cvt_pk_bf16_f32 %2, %0, %0\n\tv_mul_f32 %3, %3, %5\n\tv_lshlrev_b32 %0, 16, %2" : "+v"(xv), "+v"(xt1), "=&v"(xc), "+v"(xt2) : "v"(yf0_1), "v"(yf0_2));
  SB(); GM();
  DSW128(rw2, acc2[1][2], 0);
  asm volatile("v_mul_f32 %0, %0, %6\n\tv_rcp_f32 %1, %1\n\ts_nop 0\n\tv_fma_f32 %2, %1, %7, %2\n\tv_fma_f32 %3, %3, %1, %8\n\tv_fma_f32 %4, %4, %1, %9\n\tv_fma_f32 %5, %5, %1, %10" : "+v"(xt3), "+v"(xv), "+v"(xm), "+v"(xt0), "+v"(xt1), "+v"(xt2) : "v"(yf0_3), "v"(xy), "v"(x20_0), "v"(x20_1), "v"(x20_2));
  asm volatile("v_fma_f32 %0, %0, %2, %3\n\tv_cvt_pk_bf16_f32 %1, %4, %4" : "+v"(xt3), "=&v"(xc) : "v"(xv), "v"(x20_3), "v"(xm));
  SB(); GM();
  GLDS1(118784, sX3, 4096);
  DSW128(rw3, acc2[1][3], 0);
  asm volatile("v_lshlrev_b32 %0, 16, %6\n\tv_cvt_pk_bf16_f32 %1, %3, %4\n\tv_cvt_pk_bf16_f32 %2, %5, %7\n\tv_lshlrev_b32 %3, 16, %1\n\tv_and_b32 %4, 0xffff0000, %1\n\tv_lshlrev_b32 %5, 16, %2" : "=&v"(xmb), "=&v"(x3p2_0), "=&v"(x3p2_1), "+v"(xt0), "+v"(xt1), "+v"(xt2) : "v"(xc), "v"(xt3));
  SB(); GM();
  GLDS1(2048, sX3, 231424);
  asm volatile("v_and_b32 %0, 0xffff0000, %6\n\tv_sub_f32 %1, %1, %7\n\tv_sub_f32 %2, %2, %7\n\tv_sub_f32 %3, %3, %7\n\tv_sub_f32 %0, %0, %7\n\tv_cvt_pk_bf16_f32 %4, %1, %2\n\tv_cvt_pk_bf16_f32 %5, %3, %0" : "=&v"(xt3), "+v"(xt0), "+v"(xt1), "+v"(xt2), "=&v"(xo0), "=&v"(xo1) : "v"(x3p2_1), "v"(xmb));
  SB(); GM();
  { xo = v2u{xo0, xo1}; const unsigned so_ = sX3s + 917504u; BSTORE2(xo, v_xst, so_); }
  asm volatile("v_mul_f32 %0, %2, %2\n\tv_sub_f32 %1, %3, %4\n\tv_fma_f32 %0, %5, %5, %0\n\tv_fma_f32 %0, %6, %6, %0\n\tv_fma_f32 %0, %7, %7, %0" : "=&v"(xd2), "=&v"(dm2) : "v"(xt0), "v"(xm), "v"(xmb), "v"(xt1), "v"(xt2), "v"(xt3));
  SB(); GM();
  GLDS1(119808, sX3, 5120);
  asm volatile("v_add_f32_dpp %0, %0, %0 row_ror:8 row_mask:0xf bank_mask:0xf" : "+v"(xd2) : );
  SB(); GM();
  asm volatile("s_nop 1\n\tv_add_f32_dpp %0, %0, %0 row_ror:4 row_mask:0xf bank_mask:0xf" : "+v"(xd2) : );
  SB(); GM();
  WAITL(0);
  SBAR();
  DSR128(pr[0], red_rd, 0);
  DSR128(pr[1], red_rd, 4096);
  DSR128(pr[2], red_rd, 8192);
  DSR128(pr[3], red_rd, 12288);
  SB(); GM();
  asm volatile("v_add_f32_dpp %0, %0, %0 row_ror:2 row_mask:0xf bank_mask:0xf" : "+v"(xd2) : );
  SB(); GM();
  GLDS1(120832, sX3, 6144);
  SB(); GM();
  asm volatile("v_add_f32_dpp %0, %0, %0 row_ror:1 row_mask:0xf bank_mask:0xf" : "+v"(xd2) : );
  SB(); GM();
  WAITL(0);
  asm volatile("v_add_f32 %0, %4, %5\n\tv_add_f32 %1, %6, %7\n\tv_add_f32 %2, %8, %9\n\tv_add_f32 %3, %10, %11\n\tv_add_f32 %0, %0, %12\n\tv_add_f32 %1, %1, %13\n\tv_add_f32 %2, %2, %14" : "=&v"(fo0), "=&v"(fo1), "=&v"(fo2), "=&v"(fo3) : "v"(pr[0][0]), "v"(pr[1][0]), "v"(pr[0][1]), "v"(pr[1][1]), "v"(pr[0][2]), "v"(pr[1][2]), "v"(pr[0][3]), "v"(pr[1][3]), "v"(pr[2][0]), "v"(pr[2][1]), "v"(pr[2][2]));
  asm volatile("v_add_f32 %0, %0, %3\n\tv_add_f32 %1, %1, %4\n\tv_add_f32 %2, %2, %5" : "+v"(fo3), "+v"(fo0), "+v"(fo1) : "v"(pr[2][3]), "v"(pr[3][0]), "v"(pr[3][1]));
  SB(); GM();
  GLDS1(3072, sX3, 232448);
  asm volatile("v_add_f32 %0, %0, %5\n\tv_add_f32 %1, %1, %6\n\tv_fma_f32 %2, %2, %7, %8\n\tv_fma_f32 %3, %3, %7, %9\n\tv_fma_f32 %0, %0, %7, %10\n\tv_fma_f32 %1, %1, %7, %11\n\tv_cvt_pk_bf16_f32 %4, %2, %3" : "+v"(fo2), "+v"(fo3), "+v"(fo0), "+v"(fo1), "=&v"(fp0) : "v"(pr[3][2]), "v"(pr[3][3]), "v"(rsq3), "v"(rf0), "v"(rf1), "v"(rf2), "v"(rf3));
  asm volatile("v_cvt_pk_bf16_f32 %0, %1, %2" : "=v"(fp1) : "v"(fo2), "v"(fo3));
  SB(); GM();
  asm volatile("v_lshlrev_b32 %0, 16, %7\n\tv_and_b32 %1, 0xffff0000, %7\n\tv_lshlrev_b32 %2, 16, %8\n\tv_and_b32 %3, 0xffff0000, %8\n\tv_add_f32 %4, %0, %1\n\tv_mul_f32 %5, %0, %0\n\tv_mul_f32 %6, %0, %9" : "=&v"(x21_0), "=&v"(x21_1), "=&v"(x21_2), "=&v"(x21_3), "=&v"(fa), "=&v"(fb), "=&v"(fc) : "v"(fp0), "v"(fp1), "v"(yf1_0));
  asm volatile("v_add_f32 %0, %0, %3\n\tv_fma_f32 %1, %4, %4, %1\n\tv_fma_f32 %2, %4, %5, %2\n\tv_add_f32 %0, %0, %6" : "+v"(fa), "+v"(fb), "+v"(fc) : "v"(x21_2), "v"(x21_1), "v"(yf1_1), "v"(x21_3));
  SB(); GM();
  GLDS1(4096, sX3, 233472);
  asm volatile("v_fma_f32 %0, %3, %4, %0\n\tv_fma_f32 %1, %3, %3, %1\n\tv_fma_f32 %0, %5, %6, %0\n\tv_fma_f32 %1, %5, %5, %1\n\tv_add_f32_dpp %2, %7, %7 row_ror:8 row_mask:0xf bank_mask:0xf\n\tv_add_f32_dpp %0, %0, %0 row_ror:8 row_mask:0xf bank_mask:0xf\n\tv_add_f32_dpp %2, %1, %1 row_ror:8 row_mask:0xf bank_mask:0xc" : "+v"(fc), "+v"(fb), "=&v"(ft0) : "v"(x21_2), "v"(yf1_2), "v"(x21_3), "v"(yf1_3), "v"(fa));
  asm volatile("s_nop 1\n\tv_add_f32_dpp %0, %1, %1 row_half_mirror row_mask:0xf bank_mask:0xf" : "=v"(fu) : "v"(ft0));
  SB(); GM();
  asm volatile("v_add_f32_dpp %0, %1, %1 row_half_mirror row_mask:0xf bank_mask:0x2\n\ts_nop 1\n\tv_add_f32_dpp %0, %0, %0 quad_perm:[1,0,3,2] row_mask:0xf bank_mask:0xf\n\ts_nop 1\n\tv_add_f32_dpp %0, %0, %0 quad_perm:[2,3,0,1] row_mask:0xf bank_mask:0xf" : "+v"(fu) : "v"(fc));
  { gp[0] = f2u(fu); const unsigned so_ = sGs + 1 * GR_KIND + 344064u; BSTORE2(gp, v_gst, so_); }
  SB(); GM();
  GLDS1(5120, sX3, 234496);
  SB(); GM();
  GLDS1(6144, sX3, 235520);
  SB(); GM();
  SB(); GM();
  WAITV(7);
  { const unsigned so_ = sXf + 229376u; BSTORE2(tagv, v_one, so_); }
  { gp[0] = f2u(xd2); const unsigned so_ = sGs + 2 * GR_KIND + 229376u; BSTORE2(gp, v_gst, so_); }
  SB(); GM();
  WAITV(7);
  SBAR();
  DSR128(xf[0], xadr[0], 0);
  DSR128(xf[1], xadr[0], 4096);
  DSR128(xf[2], xadr[0], 8192);
  DSR128(xf[3], xadr[0], 12288);
  DSR128(xf[4], xadr[0], 16384);
  WAITL(4);
  MF32Z("v", acc[0], wg[0], xf[0]);
  DSR128(xf[5], xadr[0], 20480);
  asm volatile("v_add_u32 %0, %1, %2" : "=v"(xadr[1]) : "s"(sXg[1] + 114688u), "v"(xl1));
  { const unsigned so_ = sXp + 114688u; BLOAD2(pollx, v_px, so_); }
  SB(); GM();
  WAITL(4);
  MF32("v", acc[0], wg[1], xf[1]);
  DSR128(xf[6], xadr[0], 24576);
  SB(); GM();
  WAITL(4);
  MF32("v", acc[0], wg[2], xf[2]);
  DSR128(xf[7], xadr[1], 0);
  SB(); GM();
  WAITL(4);
  MF32("v", acc[0], wg[3], xf[3]);
  DSR128(xf[0], xadr[1], 4096);
  SB(); GM();
  WAITL(4);
  MF32("v", acc[0], wg[4], xf[4]);
  DSR128(xf[1], xadr[1], 8192);
  SB(); GM();
  WAITL(4);
  MF32("v", acc[0], wg[5], xf[5]);
  DSR128(xf[2], xadr[1], 12288);
  SB(); GM();
  WAITL(4);
  MF32("a", acc[0], wg[6], xf[6]);
  DSR128(xf[3], xadr[1], 16384);
  SB(); GM();
  WAITL(4);
  MF32("a", acc[0], wg[7], xf[7]);
  DSR128(xf[4], xadr[1], 20480);
  asm volatile("v_add_u32 %0, %1, %2" : "=v"(xadr[0]) : "s"(sXg[2] + 114688u), "v"(xl1));
  SB(); GM();
  WAITL(4);
  MF32("a", acc[0], wg[8], xf[0]);
  DSR128(xf[5], xadr[1], 24576);
  { const unsigned so_ = sGl + 2 * GR_KIND + 0u; GLOADG(gC, 252, 255, so_); }
  SB(); GM();
  WAITL(4);
  MF32("a", acc[0], wg[9], xf[1]);
  DSR128(xf[6], xadr[0], 0);
  SB(); GM();
  WAITL(4);
  MF32("a", acc[0], wg[10], xf[2]);
  DSR128(xf[7], xadr[0], 4096);
  SB(); GM();
  WAITL(4);
  MF32("a", acc[0], wg[11], xf[3]);
  DSR128(xf[0], xadr[0], 8192);
  SB(); GM();
  WAITL(4);
  MF32("a", acc[0], wg[12], xf[4]);
  DSR128(xf[1], xadr[0], 12288);
  SB(); GM();
  WAITL(4);
  MF32("a", acc[0], wg[13], xf[5]);
  DSR128(xf[2], xadr[0], 16384);
  SB(); GM();
  WAITL(4);
  MF32("a", acc[0], wg[14], xf[6]);
  DSR128(xf[3], xadr[0], 20480);
  asm volatile("v_add_u32 %0, %1, %2" : "=v"(xadr[1]) : "s"(sXg[3] + 114688u), "v"(xl1));
  WAITV(1);
  { const unsigned so_ = sXp + 114688u; CHECKF(pollx, v_px, so_); }
  GLDS1(28672, sX3, 458752);
  SB(); GM();
  WAITL(4);
  MF32("a", acc[0], wg[15], xf[7]);
  DSR128(xf[4], xadr[0], 24576);
  GLDS1(29696, sX3, 459776);
  SB(); GM();
  WAITL(4);
  MF32("a", acc[0], wg[16], xf[0]);
  DSR128(xf[5], xadr[1], 0);
  SB(); GM();
  WAITL(4);
  MF32("a", acc[0], wg[17], xf[1]);
  DSR128(xf[6], xadr[1], 4096);
  GLDS1(30720, sX3, 460800);
  SB(); GM();
  WAITL(4);
  MF32("a", acc[0], wg[18], xf[2]);
  DSR128(xf[7], xadr[1], 8192);
  SB(); GM();
  WAITL(4);
  MF32("a", acc[0], wg[19], xf[3]);
  DSR128(xf[0], xadr[1], 12288);
  { const unsigned so_ = sGl + 1 * GR_KIND + 344064u; GLOADG(gB, 248, 251, so_); }
  GLDS1(31744, sX3, 461824);
  SB(); GM();
  WAITL(4);
  MF32("a", acc[0], wg[20], xf[4]);
  DSR128(xf[1], xadr[1], 16384);
  SB(); GM();
  WAITL(4);
  MF32("a", acc[0], wg[21], xf[5]);
  DSR128(xf[2], xadr[1], 20480);
  asm volatile("v_add_u32 %0, %1, %2" : "=v"(xadr[0]) : "s"(sXg[0] + 0u), "v"(xl1));
  GLDS1(32768, sX3, 462848);
  SB(); GM();
  WAITL(4);
  MF32("a", acc[0], wg[22], xf[6]);
  DSR128(xf[3], xadr[1], 24576);
  WAITV(6);
  { const unsigned so_ = sGl + 2 * GR_KIND + 0u; CHECKG(gC, 252, 253, 255, so_); }
  SB(); GM();
  WAITL(4);
  MF32("a", acc[0], wg[23], xf[7]);
  SBAR();
  DSR128(xf[4], xadr[0], 0);
  GLDS1(33792, sX3, 463872);
  SB(); GM();
  WAITL(4);
  MF32("a", acc[0], wg[24], xf[0]);
  DSR128(xf[5], xadr[0], 4096);
  asm volatile("v_add_f32_dpp %0, %1, %1 row_ror:2 row_mask:0xf bank_mask:0xf\n\ts_nop 1\n\tv_add_f32_dpp %0, %0, %0 row_ror:4 row_mask:0xf bank_mask:0xf\n\ts_nop 1\n\tv_add_f32_dpp %0, %0, %0 row_ror:8 row_mask:0xf bank_mask:0xf" : "=&v"(kp) : "v"(gC[0]));
  SB(); GM();
  WAITL(4);
  MF32("a", acc[0], wg[25], xf[1]);
  DSR128(xf[6], xadr[0], 8192);
  GLDS1(34816, sX3, 464896);
  SB(); GM();
  WAITL(4);
  MF32("a", acc[0], wg[26], xf[2]);
  DSR128(xf[7], xadr[0], 12288);
  asm volatile("v_mul_f32 %0, %1, %2\n\tv_fma_f32 %0, -%3, %3, %0\n\tv_max_f32 %0, 0, %0" : "=&v"(kv) : "v"(kp), "v"(vinvD), "v"(dm0));
  SB(); GM();
  WAITL(4);
  MF32("a", acc[0], wg[27], xf[3]);
  DSR128(xf[0], xadr[0], 16384);
  asm volatile("v_cvt_pk_bf16_f32 %0, %1, %1\n\tv_lshlrev_b32 %1, 16, %0\n\tv_add_f32 %1, %2, %1" : "=&v"(kt), "+v"(kv) : "s"(sEps));
  SB(); GM();
  WAITL(4);
  MF32Z("v", acc[1], wg[0], xf[4]);
  DSR128(xf[1], xadr[0], 20480);
  asm volatile("v_add_u32 %0, %1, %2" : "=v"(xadr[1]) : "s"(sXg[1] + 0u), "v"(xl1));
  asm volatile("v_cvt_pk_bf16_f32 %0, %1, %1\n\tv_lshlrev_b32 %1, 16, %0" : "=&v"(kt), "+v"(kv) : );
  SB(); GM();
  WAITL(4);
  MF32("v", acc[1], wg[1], xf[5]);
  DSR128(xf[2], xadr[0], 24576);
  SBAR();
  GLDS1(114688, sX3, 688128);
  SB(); GM();
  WAITL(4);
  MF32("v", acc[1], wg[2], xf[6]);
  DSR128(xf[3], xadr[1], 0);
  asm volatile("v_sqrt_f32 %0, %0\n\ts_nop 0\n\tv_cvt_pk_bf16_f32 %1, %0, %0" : "+v"(kv), "=&v"(kt) : );
  SB(); GM();
  WAITL(4);
  MF32("v", acc[1], wg[3], xf[7]);
  DSR128(xf[4], xadr[1], 4096);
  GLDS1(115712, sX3, 689152);
  asm volatile("v_max_f32 %0, 0, %1" : "=v"(ep0) : "v"(acc[0][0]));
  SB(); GM();
  WAITL(4);
  MF32("v", acc[1], wg[4], xf[0]);
  DSR128(xf[5], xadr[1], 8192);
  asm volatile("v_max_f32 %0, 0, %3\n\tv_mul_f32 %1, %1, %4\n\tv_mul_f32 %0, %0, %5\n\tv_cvt_pk_bf16_f32 %2, %1, %0" : "=&v"(ep1), "+v"(ep0), "=&v"(hp0) : "v"(acc[0][1]), "v"(acc[0][8]), "v"(acc[0][9]));
  SB(); GM();
  WAITL(4);
  MF32("v", acc[1], wg[5], xf[1]);
  DSR128(xf[6], xadr[1], 12288);
  GLDS1(116736, sX3, 690176);
  asm volatile("v_max_f32 %0, 0, %1" : "=v"(ep0) : "v"(acc[0][2]));
  SB(); GM();
  WAITL(4);
  MF32("a", acc[1], wg[6], xf[2]);
  DSR128(xf[7], xadr[1], 16384);
  asm volatile("v_max_f32 %0, 0, %3\n\tv_mul_f32 %1, %1, %4\n\tv_mul_f32 %0, %0, %5\n\tv_cvt_pk_bf16_f32 %2, %1, %0" : "=&v"(ep1), "+v"(ep0), "=&v"(hp1) : "v"(acc[0][3]), "v"(acc[0][10]), "v"(acc[0][11]));
  SB(); GM();
  WAITL(4);
  MF32("a", acc[1], wg[7], xf[3]);
  DSR128(xf[0], xadr[1], 20480);
  asm volatile("v_add_u32 %0, %1, %2" : "=v"(xadr[0]) : "s"(sXg[2] + 0u), "v"(xl1));
  GLDS1(117760, sX3, 691200);
  SB(); GM();
  WAITL(4);
  MF32("a", acc[1], wg[8], xf[4]);
  DSR128(xf[1], xadr[1], 24576);
  asm volatile("v_max_f32 %0, 0, %2\n\tv_max_f32 %1, 0, %3\n\tv_mul_f32 %0, %0, %4\n\tv_mul_f32 %1, %1, %5" : "=&v"(ep0), "=&v"(ep1) : "v"(acc[0][4]), "v"(acc[0][5]), "v"(acc[0][12]), "v"(acc[0][13]));
  SB(); GM();
  WAITL(4);
  MF32("a", acc[1], wg[9], xf[5]);
  DSR128(xf[2], xadr[0], 0);
  GLDS1(118784, sX3, 692224);
  asm volatile("v_cvt_pk_bf16_f32 %0, %1, %2" : "=v"(hp2) : "v"(ep0), "v"(ep1));
  SB(); GM();
  WAITL(4);
  MF32("a", acc[1], wg[10], xf[6]);
  DSR128(xf[3], xadr[0], 4096);
  asm volatile("v_max_f32 %0, 0, %2\n\tv_max_f32 %1, 0, %3\n\tv_mul_f32 %0, %0, %4\n\tv_mul_f32 %1, %1, %5" : "=&v"(ep0), "=&v"(ep1) : "v"(acc[0][6]), "v"(acc[0][7]), "v"(acc[0][14]), "v"(acc[0][15]));
  SB(); GM();
  WAITL(4);
  MF32("a", acc[1], wg[11], xf[7]);
  DSR128(xf[4], xadr[0], 8192);
  GLDS1(119808, sX3, 693248);
  asm volatile("v_cvt_pk_bf16_f32 %0, %1, %2" : "=v"(hp3) : "v"(ep0), "v"(ep1));
  SB(); GM();
  WAITL(4);
  MF32("a", acc[1], wg[12], xf[0]);
  DSR128(xf[5], xadr[0], 12288);
  { hfr = v4u{hp0, hp1, hp2, hp3}; const unsigned so_ = sHst + 1 * H_LAYER + 0u; BSTORE4(hfr, v_hst, so_); }
  SB(); GM();
  WAITL(4);
  MF32("a", acc[1], wg[13], xf[1]);
  DSR128(xf[6], xadr[0], 16384);
  GLDS1(120832, sX3, 694272);
  SB(); GM();
  WAITL(4);
  MF32("a", acc[1], wg[14], xf[2]);
  DSR128(xf[7], xadr[0], 20480);
  asm volatile("v_add_u32 %0, %1, %2" : "=v"(xadr[1]) : "s"(sXg[3] + 0u), "v"(xl1));
  asm volatile("v_lshlrev_b32 %0, 16, %1" : "=v"(kv) : "v"(kt));
  SB(); GM();
  WAITL(4);
  MF32("a", acc[1], wg[15], xf[3]);
  DSR128(xf[0], xadr[0], 24576);
  asm volatile("v_rcp_f32 %0, %0\n\ts_nop 0\n\tv_mul_f32 %1, %0, %0" : "+v"(kv), "=&v"(rsq4) : );
  SB(); GM();
  WAITL(4);
  MF32("a", acc[1], wg[16], xf[4]);
  DSR128(xf[1], xadr[1], 0);
  WAITV(12);
  { const unsigned so_ = sGl + 1 * GR_KIND + 344064u; CHECKG(gB, 248, 249, 251, so_); }
  SB(); GM();
  WAITL(4);
  MF32("a", acc[1], wg[17], xf[5]);
  DSR128(xf[2], xadr[1], 4096);
  asm volatile("v_add_f32_dpp %0, %2, %2 row_ror:2 row_mask:0xf bank_mask:0xf\n\tv_add_f32_dpp %1, %3, %3 row_ror:2 row_mask:0xf bank_mask:0xf\n\ts_nop 0\n\tv_add_f32_dpp %0, %0, %0 row_ror:4 row_mask:0xf bank_mask:0xf\n\tv_add_f32_dpp %1, %1, %1 row_ror:4 row_mask:0xf bank_mask:0xf\n\ts_nop 0\n\tv_add_f32_dpp %0, %0, %0 row_ror:8 row_mask:0xf bank_mask:0xf" : "=&v"(xp), "=&v"(xq) : "v"(gB[0]), "v"(gB[2]));
  asm volatile("v_add_f32_dpp %0, %0, %0 row_ror:8 row_mask:0xf bank_mask:0xf" : "+v"(xq) : );
  SB(); GM();
  WAITL(4);
  MF32("a", acc[1], wg[18], xf[6]);
  DSR128(xf[3], xadr[1], 8192);
  asm volatile("v_mul_f32_dpp %0, %5, %6 quad_perm:[0,0,2,2] row_mask:0xf bank_mask:0xf\n\tv_mul_f32_dpp %1, %5, %6 quad_perm:[1,1,3,3] row_mask:0xf bank_mask:0xf\n\tv_mul_f32_dpp %2, %7, %6 quad_perm:[0,0,2,2] row_mask:0xf bank_mask:0xf\n\tv_fma_f32 %1, -%0, %0, %1\n\tv_cvt_pk_bf16_f32 %3, %0, %0\n\tv_lshlrev_b32 %4, 16, %3" : "=&v"(xm), "=&v"(xv), "=&v"(xy), "=&v"(xc), "=&v"(xmb) : "v"(xp), "v"(vinvD), "v"(xq));
  SB(); GM();
  WAITL(4);
  MF32("a", acc[1], wg[19], xf[7]);
  DSR128(xf[4], xadr[1], 12288);
  { const unsigned so_ = sXp + 229376u; BLOAD2(pollx, v_px, so_); }
  asm volatile("v_max_f32 %0, 0, %0\n\tv_sub_f32 %1, %4, %5\n\tv_cvt_pk_bf16_f32 %2, %0, %0\n\tv_sub_f32 %3, %6, %5" : "+v"(xv), "=&v"(xt0), "=&v"(xc), "=&v"(xt1) : "v"(x21_0), "v"(xmb), "v"(x21_1));
  SB(); GM();
  WAITL(4);
  MF32("a", acc[1], wg[20], xf[0]);
  DSR128(xf[5], xadr[1], 16384);
  asm volatile("v_lshlrev_b32 %0, 16, %3\n\tv_sub_f32 %1, %5, %6\n\tv_add_f32 %0, %7, %0\n\tv_sub_f32 %2, %8, %6\n\tv_cvt_pk_bf16_f32 %3, %0, %0\n\tv_fma_f32 %4, -%6, %9, %4" : "=&v"(xv), "=&v"(xt2), "=&v"(xt3), "+v"(xc), "+v"(xy) : "v"(x21_2), "v"(xmb), "s"(sEps), "v"(x21_3), "v"(sy3));
  SB(); GM();
  WAITL(4);
  MF32("a", acc[1], wg[21], xf[1]);
  DSR128(xf[6], xadr[1], 20480);
  asm volatile("v_add_u32 %0, %1, %2" : "=v"(xadr[0]) : "s"(sXg[0] + 28672u), "v"(xl1));
  asm volatile("v_lshlrev_b32 %0, 16, %3\n\tv_mul_f32 %1, %1, %4\n\tv_sqrt_f32 %0, %0\n\tv_mul_f32 %2, %2, %5" : "=&v"(xv), "+v"(xt0), "+v"(xt1) : "v"(xc), "v"(yf1_0), "v"(yf1_1));
  SB(); GM();
  WAITL(4);
  MF32("a", acc[1], wg[22], xf[2]);
  DSR128(xf[7], xadr[1], 24576);
  asm volatile("v_cvt_pk_bf16_f32 %0, %2, %2\n\tv_mul_f32 %1, %1, %4\n\tv_lshlrev_b32 %2, 16, %0\n\tv_mul_f32 %3, %3, %5\n\tv_rcp_f32 %2, %2" : "=&v"(xc), "+v"(xt2), "+v"(xv), "+v"(xt3) : "v"(yf1_2), "v"(yf1_3));
  SB(); GM();
  WAITL(4);
  MF32("a", acc[1], wg[23], xf[3]);
  WAITV(9);
  SBAR();
  DSR128(xf[0], xadr[0], 0);
  asm volatile("v_fma_f32 %0, %4, %5, %0\n\tv_fma_f32 %1, %1, %4, %6\n\tv_fma_f32 %2, %2, %4, %7\n\tv_fma_f32 %3, %3, %4, %8" : "+v"(xm), "+v"(xt0), "+v"(xt1), "+v"(xt2) : "v"(xv), "v"(xy), "v"(x21_0), "v"(x21_1), "v"(x21_2));
  SB(); GM();
  WAITL(4);
  MF32("a", acc[1], wg[24], xf[4]);
  DSR128(xf[1], xadr[0], 4096);
  asm volatile("v_fma_f32 %0, %0, %6, %7\n\tv_cvt_pk_bf16_f32 %1, %8, %8\n\tv_lshlrev_b32 %2, 16, %1\n\tv_cvt_pk_bf16_f32 %3, %5, %9\n\tv_cvt_pk_bf16_f32 %4, %10, %0\n\tv_lshlrev_b32 %5, 16, %3" : "+v"(xt3), "=&v"(xc), "=&v"(xmb), "=&v"(x3p3_0), "=&v"(x3p3_1), "+v"(xt0) : "v"(xv), "v"(x21_3), "v"(xm), "v"(xt1), "v"(xt2));
  SB(); STUB(14, ".Lwret14"); GM();
  WAITL(4);
  MF32("a", acc[1], wg[25], xf[5]);
  DSR128(xf[2], xadr[0], 8192);
  asm volatile("v_and_b32 %0, 0xffff0000, %4\n\tv_lshlrev_b32 %1, 16, %5\n\tv_and_b32 %2, 0xffff0000, %5\n\tv_sub_f32 %3, %3, %6\n\tv_sub_f32 %0, %0, %6\n\tv_sub_f32 %1, %1, %6" : "=&v"(xt1), "=&v"(xt2), "=&v"(xt3), "+v"(xt0) : "v"(x3p3_0), "v"(x3p3_1), "v"(xmb));
  SB(); GM();
  WAITL(4);
  MF32("a", acc[1], wg[26], xf[6]);
  DSR128(xf[3], xadr[0], 12288);
  asm volatile("v_sub_f32 %0, %0, %3\n\tv_cvt_pk_bf16_f32 %1, %4, %5\n\tv_cvt_pk_bf16_f32 %2, %6, %0" : "+v"(xt3), "=&v"(xo0), "=&v"(xo1) : "v"(xmb), "v"(xt0), "v"(xt1), "v"(xt2));
  { xo = v2u{xo0, xo1}; const unsigned so_ = sX3s + 1376256u; BSTORE2(xo, v_xst, so_); }
  asm volatile("v_mul_f32 %0, %1, %1" : "=v"(xd3) : "v"(xt0));
  SB(); GM();
  WAITL(4);
  MF32("a", acc[1], wg[27], xf[7]);
  DSR128(xf[4], xadr[0], 16384);
  asm volatile("v_sub_f32 %0, %2, %3\n\tv_fma_f32 %1, %4, %4, %1\n\tv_fma_f32 %1, %5, %5, %1\n\tv_fma_f32 %1, %6, %6, %1" : "=&v"(dm3), "+v"(xd3) : "v"(xm), "v"(xmb), "v"(xt1), "v"(xt2), "v"(xt3));
  SB(); GM();
  WAITL(4);
  MF32Z("v", acc[0], wg[0], xf[0]);
  DSR128(xf[5], xadr[0], 20480);
  asm volatile("v_add_u32 %0, %1, %2" : "=v"(xadr[1]) : "s"(sXg[1] + 28672u), "v"(xl1));
  asm volatile("v_add_f32_dpp %0, %0, %0 row_ror:8 row_mask:0xf bank_mask:0xf" : "+v"(xd3) : );
  SB(); GM();
  WAITL(4);
  MF32("v", acc[0], wg[1], xf[1]);
  DSR128(xf[6], xadr[0], 24576);
  asm volatile("v_add_f32_dpp %0, %0, %0 row_ror:4 row_mask:0xf bank_mask:0xf" : "+v"(xd3) : );
  SB(); GM();
  WAITL(4);
  MF32("v", acc[0], wg[2], xf[2]);
  DSR128(xf[7], xadr[1], 0);
  asm volatile("v_add_f32_dpp %0, %0, %0 row_ror:2 row_mask:0xf bank_mask:0xf" : "+v"(xd3) : );
  SB(); GM();
  WAITL(4);
  MF32("v", acc[0], wg[3], xf[3]);
  DSR128(xf[0], xadr[1], 4096);
  asm volatile("v_max_f32 %0, 0, %3\n\tv_max_f32 %1, 0, %4\n\tv_mul_f32 %0, %0, %5\n\tv_mul_f32 %1, %1, %6\n\tv_cvt_pk_bf16_f32 %2, %0, %1\n\tv_max_f32 %0, 0, %7" : "=&v"(ep0), "=&v"(ep1), "=&v"(hp0) : "v"(acc[1][0]), "v"(acc[1][1]), "v"(acc[1][8]), "v"(acc[1][9]), "v"(acc[1][2]));
  SB(); GM();
  WAITL(4);
  MF32("v", acc[0], wg[4], xf[4]);
  DSR128(xf[1], xadr[1], 8192);
  asm volatile("v_max_f32 %0, 0, %3\n\tv_mul_f32 %1, %1, %4\n\tv_mul_f32 %0, %0, %5\n\tv_cvt_pk_bf16_f32 %2, %1, %0\n\tv_max_f32 %1, 0, %6\n\tv_max_f32 %0, 0, %7" : "=&v"(ep1), "+v"(ep0), "=&v"(hp1) : "v"(acc[1][3]), "v"(acc[1][10]), "v"(acc[1][11]), "v"(acc[1][4]), "v"(acc[1][5]));
  SB(); GM();
  WAITL(4);
  MF32("v", acc[0], wg[5], xf[5]);
  DSR128(xf[2], xadr[1], 12288);
  asm volatile("v_mul_f32 %0, %0, %3\n\tv_mul_f32 %1, %1, %4\n\tv_cvt_pk_bf16_f32 %2, %0, %1\n\tv_max_f32 %0, 0, %5\n\tv_max_f32 %1, 0, %6\n\tv_mul_f32 %0, %0, %7" : "+v"(ep0), "+v"(ep1), "=&v"(hp2) : "v"(acc[1][12]), "v"(acc[1][13]), "v"(acc[1][6]), "v"(acc[1][7]), "v"(acc[1][14]));
  SB(); GM();
  WAITL(4);
  MF32("a", acc[0], wg[6], xf[6]);
  DSR128(xf[3], xadr[1], 16384);
  WAITV(3);
  { const unsigned so_ = sHf + 917504u; BSTORE2(tagv, v_one, so_); }
  asm volatile("v_mul_f32 %0, %0, %3\n\tv_cvt_pk_bf16_f32 %1, %4, %0\n\tv_add_f32_dpp %2, %2, %2 row_ror:1 row_mask:0xf bank_mask:0xf" : "+v"(ep1), "=&v"(hp3), "+v"(xd3) : "v"(acc[1][15]), "v"(ep0));
  SB(); GM();
  WAITL(4);
  MF32("a", acc[0], wg[7], xf[7]);
  DSR128(xf[4], xadr[1], 20480);
  asm volatile("v_add_u32 %0, %1, %2" : "=v"(xadr[0]) : "s"(sXg[2] + 28672u), "v"(xl1));
  { hfr = v4u{hp0, hp1, hp2, hp3}; const unsigned so_ = sHst + 1 * H_LAYER + 917504u; BSTORE4(hfr, v_hst, so_); }
  SB(); GM();
  WAITL(4);
  MF32("a", acc[0], wg[8], xf[0]);
  DSR128(xf[5], xadr[1], 24576);
  WAITV(3);
  { const unsigned so_ = sXp + 229376u; CHECKF(pollx, v_px, so_); }
  SBAR();
  SB(); GM();
  WAITL(4);
  MF32("a", acc[0], wg[9], xf[1]);
  DSR128(xf[6], xadr[0], 0);
  GLDS1(0, sX3, 917504);
  SB(); GM();
  WAITL(4);
  MF32("a", acc[0], wg[10], xf[2]);
  DSR128(xf[7], xadr[0], 4096);
  SB(); GM();
  WAITL(4);
  MF32("a", acc[0], wg[11], xf[3]);
  DSR128(xf[0], xadr[0], 8192);
  GLDS1(1024, sX3, 918528);
  SB(); GM();
  WAITL(4);
  MF32("a", acc[0], wg[12], xf[4]);
  DSR128(xf[1], xadr[0], 12288);
  WAITV(4);
  { const unsigned so_ = sXf + 344064u; BSTORE2(tagv, v_one, so_); }
  { gp[0] = f2u(xd3); const unsigned so_ = sGs + 2 * GR_KIND + 344064u; BSTORE2(gp, v_gst, so_); }
  SB(); GM();
  WAITL(4);
  MF32("a", acc[0], wg[13], xf[5]);
  DSR128(xf[2], xadr[0], 16384);
  GLDS1(2048, sX3, 919552);
  SB(); GM();
  WAITL(4);
  MF32("a", acc[0], wg[14], xf[6]);
  DSR128(xf[3], xadr[0], 20480);
  asm volatile("v_add_u32 %0, %1, %2" : "=v"(xadr[1]) : "s"(sXg[3] + 28672u), "v"(xl1));
  SB(); GM();
  WAITL(4);
  MF32("a", acc[0], wg[15], xf[7]);
  DSR128(xf[4], xadr[0], 24576);
  GLDS1(3072, sX3, 920576);
  SB(); GM();
  WAITL(4);
  MF32("a", acc[0], wg[16], xf[0]);
  DSR128(xf[5], xadr[1], 0);
  { const unsigned so_ = sGl + 2 * GR_KIND + 114688u; GLOADG(gC, 252, 255, so_); }
  SB(); GM();
  WAITL(4);
  MF32("a", acc[0], wg[17], xf[1]);
  DSR128(xf[6], xadr[1], 4096);
  GLDS1(4096, sX3, 921600);
  SB(); GM();
  WAITL(4);
  MF32("a", acc[0], wg[18], xf[2]);
  DSR128(xf[7], xadr[1], 8192);
  SB(); GM();
  WAITL(4);
  MF32("a", acc[0], wg[19], xf[3]);
  DSR128(xf[0], xadr[1], 12288);
  GLDS1(5120, sX3, 922624);
  SB(); GM();
  WAITL(4);
  MF32("a", acc[0], wg[20], xf[4]);
  DSR128(xf[1], xadr[1], 16384);
  SB(); GM();
  WAITL(4);
  MF32("a", acc[0], wg[21], xf[5]);
  DSR128(xf[2], xadr[1], 20480);
  asm volatile("v_add_u32 %0, %1, %2" : "=v"(xadr[0]) : "s"(sXg[0] + 114688u), "v"(xl1));
  GLDS1(6144, sX3, 923648);
  SB(); GM();
  WAITL(4);
  MF32("a", acc[0], wg[22], xf[6]);
  DSR128(xf[3], xadr[1], 24576);
  SB(); GM();
  WAITL(4);
  MF32("a", acc[0], wg[23], xf[7]);
  SBAR();
  DSR128(xf[4], xadr[0], 0);
  WAITV(10);
  { const unsigned so_ = sHf + 1032192u; BSTORE2(tagv, v_one, so_); }
  SB(); GM();
  WAITL(4);
  MF32("a", acc[0], wg[24], xf[0]);
  DSR128(xf[5], xadr[0], 4096);
  SB(); GM();
  WAITL(4);
  MF32("a", acc[0], wg[25], xf[1]);
  DSR128(xf[6], xadr[0], 8192);
  SB(); GM();
  WAITL(4);
  MF32("a", acc[0], wg[26], xf[2]);
  DSR128(xf[7], xadr[0], 12288);
  SB(); GM();
  WAITL(4);
  MF32("a", acc[0], wg[27], xf[3]);
  DSR128(xf[0], xadr[0], 16384);
  SB(); GM();
  WAITL(4);
  MF32Z("v", acc[1], wg[0], xf[4]);
  DSR128(xf[1], xadr[0], 20480);
  asm volatile("v_add_u32 %0, %1, %2" : "=v"(xadr[1]) : "s"(sXg[1] + 114688u), "v"(xl1));
  SB(); GM();
  WAITL(4);
  MF32("v", acc[1], wg[1], xf[5]);
  DSR128(xf[2], xadr[0], 24576);
  SBAR();
  GLDS1(28672, sX3, 1146880);
  SB(); GM();
  WAITL(4);
  MF32("v", acc[1], wg[2], xf[6]);
  DSR128(xf[3], xadr[1], 0);
  SB(); GM();
  WAITL(4);
  MF32("v", acc[1], wg[3], xf[7]);
  DSR128(xf[4], xadr[1], 4096);
  GLDS1(29696, sX3, 1147904);
  asm volatile("v_max_f32 %0, 0, %1" : "=v"(ep0) : "v"(acc[0][0]));
  SB(); GM();
  WAITL(4);
  MF32("v", acc[1], wg[4], xf[0]);
  DSR128(xf[5], xadr[1], 8192);
  asm volatile("v_max_f32 %0, 0, %3\n\tv_mul_f32 %1, %1, %4\n\tv_mul_f32 %0, %0, %5\n\tv_cvt_pk_bf16_f32 %2, %1, %0" : "=&v"(ep1), "+v"(ep0), "=&v"(hp0) : "v"(acc[0][1]), "v"(acc[0][8]), "v"(acc[0][9]));
  SB(); GM();
  WAITL(4);
  MF32("v", acc[1], wg[5], xf[1]);
  DSR128(xf[6], xadr[1], 12288);
  GLDS1(30720, sX3, 1148928);
  asm volatile("v_max_f32 %0, 0, %1" : "=v"(ep0) : "v"(acc[0][2]));
  SB(); GM();
  WAITL(4);
  MF32("a", acc[1], wg[6], xf[2]);
  DSR128(xf[7], xadr[1], 16384);
  asm volatile("v_max_f32 %0, 0, %3\n\tv_mul_f32 %1, %1, %4\n\tv_mul_f32 %0, %0, %5\n\tv_cvt_pk_bf16_f32 %2, %1, %0" : "=&v"(ep1), "+v"(ep0), "=&v"(hp1) : "v"(acc[0][3]), "v"(acc[0][10]), "v"(acc[0][11]));
  SB(); GM();
  WAITL(4);
  MF32("a", acc[1], wg[7], xf[3]);
  DSR128(xf[0], xadr[1], 20480);
  asm volatile("v_add_u32 %0, %1, %2" : "=v"(xadr[0]) : "s"(sXg[2] + 114688u), "v"(xl1));
  GLDS1(31744, sX3, 1149952);
  SB(); GM();
  WAITL(4);
  MF32("a", acc[1], wg[8], xf[4]);
  DSR128(xf[1], xadr[1], 24576);
  asm volatile("v_max_f32 %0, 0, %2\n\tv_max_f32 %1, 0, %3\n\tv_mul_f32 %0, %0, %4\n\tv_mul_f32 %1, %1, %5" : "=&v"(ep0), "=&v"(ep1) : "v"(acc[0][4]), "v"(acc[0][5]), "v"(acc[0][12]), "v"(acc[0][13]));
  SB(); GM();
  WAITL(4);
  MF32("a", acc[1], wg[9], xf[5]);
  DSR128(xf[2], xadr[0], 0);
  GLDS1(32768, sX3, 1150976);
  asm volatile("v_cvt_pk_bf16_f32 %0, %1, %2" : "=v"(hp2) : "v"(ep0), "v"(ep1));
  SB(); GM();
  WAITL(4);
  MF32("a", acc[1], wg[10], xf[6]);
  DSR128(xf[3], xadr[0], 4096);
  asm volatile("v_max_f32 %0, 0, %2\n\tv_max_f32 %1, 0, %3\n\tv_mul_f32 %0, %0, %4\n\tv_mul_f32 %1, %1, %5" : "=&v"(ep0), "=&v"(ep1) : "v"(acc[0][6]), "v"(acc[0][7]), "v"(acc[0][14]), "v"(acc[0][15]));
  SB(); GM();
  WAITL(4);
  MF32("a", acc[1], wg[11], xf[7]);
  DSR128(xf[4], xadr[0], 8192);
  GLDS1(33792, sX3, 1152000);
  asm volatile("v_cvt_pk_bf16_f32 %0, %1, %2" : "=v"(hp3) : "v"(ep0), "v"(ep1));
  SB(); GM();
  WAITL(4);
  MF32("a", acc[1], wg[12], xf[0]);
  DSR128(xf[5], xadr[0], 12288);
  { hfr = v4u{hp0, hp1, hp2, hp3}; const unsigned so_ = sHst + 1 * H_LAYER + 1835008u; BSTORE4(hfr, v_hst, so_); }
  SB(); GM();
  WAITL(4);
  MF32("a", acc[1], wg[13], xf[1]);
  DSR128(xf[6], xadr[0], 16384);
  GLDS1(34816, sX3, 1153024);
  SB(); GM();
  WAITL(4);
  MF32("a", acc[1], wg[14], xf[2]);
  DSR128(xf[7], xadr[0], 20480);
  asm volatile("v_add_u32 %0, %1, %2" : "=v"(xadr[1]) : "s"(sXg[3] + 114688u), "v"(xl1));
  SB(); GM();
  WAITL(4);
  MF32("a", acc[1], wg[15], xf[3]);
  DSR128(xf[0], xadr[0], 24576);
  WAITV(12);
  { const unsigned so_ = sGl + 2 * GR_KIND + 114688u; CHECKG(gC, 252, 253, 255, so_); }
  SB(); GM();
  WAITL(4);
  MF32("a", acc[1], wg[16], xf[4]);
  DSR128(xf[1], xadr[1], 0);
  asm volatile("v_add_f32_dpp %0, %1, %1 row_ror:2 row_mask:0xf bank_mask:0xf\n\ts_nop 1\n\tv_add_f32_dpp %0, %0, %0 row_ror:4 row_mask:0xf bank_mask:0xf\n\ts_nop 1\n\tv_add_f32_dpp %0, %0, %0 row_ror:8 row_mask:0xf bank_mask:0xf" : "=&v"(kp) : "v"(gC[0]));
  SB(); GM();
  WAITL(4);
  MF32("a", acc[1], wg[17], xf[5]);
  DSR128(xf[2], xadr[1], 4096);
  asm volatile("v_mul_f32 %0, %1, %2\n\tv_fma_f32 %0, -%3, %3, %0\n\tv_max_f32 %0, 0, %0" : "=&v"(kv) : "v"(kp), "v"(vinvD), "v"(dm1));
  SB(); GM();
  WAITL(4);
  MF32("a", acc[1], wg[18], xf[6]);
  DSR128(xf[3], xadr[1], 8192);
  asm volatile("v_cvt_pk_bf16_f32 %0, %1, %1\n\tv_lshlrev_b32 %1, 16, %0\n\tv_add_f32 %1, %2, %1" : "=&v"(kt), "+v"(kv) : "s"(sEps));
  SB(); GM();
  WAITL(4);
  MF32("a", acc[1], wg[19], xf[7]);
  DSR128(xf[4], xadr[1], 12288);
  asm volatile("v_cvt_pk_bf16_f32 %0, %1, %1\n\tv_lshlrev_b32 %1, 16, %0" : "=&v"(kt), "+v"(kv) : );
  SB(); GM();
  WAITL(4);
  MF32("a", acc[1], wg[20], xf[0]);
  DSR128(xf[5], xadr[1], 16384);
  asm volatile("v_sqrt_f32 %0, %0\n\ts_nop 0\n\tv_cvt_pk_bf16_f32 %1, %0, %0" : "+v"(kv), "=&v"(kt) : );
  SB(); GM();
  WAITL(4);
  MF32("a", acc[1], wg[21], xf[1]);
  DSR128(xf[6], xadr[1], 20480);
  asm volatile("v_add_u32 %0, %1, %2" : "=v"(xadr[0]) : "s"(sXg[0] + 0u), "v"(xl1));
  asm volatile("v_lshlrev_b32 %0, 16, %1" : "=v"(kv) : "v"(kt));
  SB(); GM();
  WAITL(4);
  MF32("a", acc[1], wg[22], xf[2]);
  DSR128(xf[7], xadr[1], 24576);
  asm volatile("v_rcp_f32 %0, %0\n\ts_nop 0\n\tv_mul_f32 %1, %0, %0" : "+v"(kv), "=&v"(rsq5) : );
  SB(); GM();
  WAITL(4);
  MF32("a", acc[1], wg[23], xf[3]);
  WAITV(9);
  SBAR();
  DSR128(xf[0], xadr[0], 0);
  SB(); GM();
  WAITL(4);
  MF32("a", acc[1], wg[24], xf[4]);
  DSR128(xf[1], xadr[0], 4096);
  SB(); GM();
  WAITL(4);
  MF32("a", acc[1], wg[25], xf[5]);
  DSR128(xf[2], xadr[0], 8192);
  SB(); GM();
  WAITL(4);
  MF32("a", acc[1], wg[26], xf[6]);
  DSR128(xf[3], xadr[0], 12288);
  SB(); GM();
  WAITL(4);
  MF32("a", acc[1], wg[27], xf[7]);
  DSR128(xf[4], xadr[0], 16384);
  SB(); GM();
  WAITL(4);
  MF32Z("v", acc[0], wg[0], xf[0]);
  DSR128(xf[5], xadr[0], 20480);
  asm volatile("v_add_u32 %0, %1, %2" : "=v"(xadr[1]) : "s"(sXg[1] + 0u), "v"(xl1));
  SB(); GM();
  WAITL(4);
  MF32("v", acc[0], wg[1], xf[1]);
  DSR128(xf[6], xadr[0], 24576);
  SB(); GM();
  WAITL(4);
  MF32("v", acc[0], wg[2], xf[2]);
  DSR128(xf[7], xadr[1], 0);
  SB(); GM();
  WAITL(4);
  MF32("v", acc[0], wg[3], xf[3]);
  DSR128(xf[0], xadr[1], 4096);
  asm volatile("v_max_f32 %0, 0, %3\n\tv_max_f32 %1, 0, %4\n\tv_mul_f32 %0, %0, %5\n\tv_mul_f32 %1, %1, %6\n\tv_cvt_pk_bf16_f32 %2, %0, %1\n\tv_max_f32 %0, 0, %7" : "=&v"(ep0), "=&v"(ep1), "=&v"(hp0) : "v"(acc[1][0]), "v"(acc[1][1]), "v"(acc[1][8]), "v"(acc[1][9]), "v"(acc[1][2]));
  SB(); GM();
  WAITL(4);
  MF32("v", acc[0], wg[4], xf[4]);
  DSR128(xf[1], xadr[1], 8192);
  asm volatile("v_max_f32 %0, 0, %3\n\tv_mul_f32 %1, %1, %4\n\tv_mul_f32 %0, %0, %5\n\tv_cvt_pk_bf16_f32 %2, %1, %0\n\tv_max_f32 %1, 0, %6\n\tv_max_f32 %0, 0, %7" : "=&v"(ep1), "+v"(ep0), "=&v"(hp1) : "v"(acc[1][3]), "v"(acc[1][10]), "v"(acc[1][11]), "v"(acc[1][4]), "v"(acc[1][5]));
  SB(); GM();
  WAITL(4);
  MF32("v", acc[0], wg[5], xf[5]);
  DSR128(xf[2], xadr[1], 12288);
  asm volatile("v_mul_f32 %0, %0, %3\n\tv_mul_f32 %1, %1, %4\n\tv_cvt_pk_bf16_f32 %2, %0, %1\n\tv_max_f32 %0, 0, %5\n\tv_max_f32 %1, 0, %6\n\tv_mul_f32 %0, %0, %7" : "+v"(ep0), "+v"(ep1), "=&v"(hp2) : "v"(acc[1][12]), "v"(acc[1][13]), "v"(acc[1][6]), "v"(acc[1][7]), "v"(acc[1][14]));
  SB(); GM();
  WAITL(4);
  MF32("a", acc[0], wg[6], xf[6]);
  DSR128(xf[3], xadr[1], 16384);
  asm volatile("v_mul_f32 %0, %0, %2\n\tv_cvt_pk_bf16_f32 %1, %3, %0" : "+v"(ep1), "=&v"(hp3) : "v"(acc[1][15]), "v"(ep0));
  { hfr = v4u{hp0, hp1, hp2, hp3}; const unsigned so_ = sHst + 1 * H_LAYER + 2752512u; BSTORE4(hfr, v_hst, so_); }
  SB(); GM();
  WAITL(4);
  MF32("a", acc[0], wg[7], xf[7]);
  DSR128(xf[4], xadr[1], 20480);
  asm volatile("v_add_u32 %0, %1, %2" : "=v"(xadr[0]) : "s"(sXg[2] + 0u), "v"(xl1));
  SB(); GM();
  WAITL(4);
  MF32("a", acc[0], wg[8], xf[0]);
  DSR128(xf[5], xadr[1], 24576);
  { const unsigned so_ = sGl + 2 * GR_KIND + 229376u; GLOADG(gC, 252, 255, so_); }
  SB(); GM();
  WAITL(4);
  MF32("a", acc[0], wg[9], xf[1]);
  DSR128(xf[6], xadr[0], 0);
  { const unsigned so_ = sXp + 344064u; BLOAD2(pollx, v_px, so_); }
  SB(); GM();
  WAITL(4);
  MF32("a", acc[0], wg[10], xf[2]);
  DSR128(xf[7], xadr[0], 4096);
  WAITV(4);
  { const unsigned so_ = sHf + 1146880u; BSTORE2(tagv, v_one, so_); }
  SB(); GM();
  WAITL(4);
  MF32("a", acc[0], wg[11], xf[3]);
  DSR128(xf[0], xadr[0], 8192);
  SB(); GM();
  WAITL(4);
  MF32("a", acc[0], wg[12], xf[4]);
  DSR128(xf[1], xadr[0], 12288);
  SB(); GM();
  WAITL(4);
  MF32("a", acc[0], wg[13], xf[5]);
  DSR128(xf[2], xadr[0], 16384);
  SB(); GM();
  WAITL(4);
  MF32("a", acc[0], wg[14], xf[6]);
  DSR128(xf[3], xadr[0], 20480);
  asm volatile("v_add_u32 %0, %1, %2" : "=v"(xadr[1]) : "s"(sXg[3] + 0u), "v"(xl1));
  SB(); GM();
  WAITL(4);
  MF32("a", acc[0], wg[15], xf[7]);
  DSR128(xf[4], xadr[0], 24576);
  SB(); GM();
  WAITL(4);
  MF32("a", acc[0], wg[16], xf[0]);
  DSR128(xf[5], xadr[1], 0);
  SB(); GM();
  WAITL(4);
  MF32("a", acc[0], wg[17], xf[1]);
  DSR128(xf[6], xadr[1], 4096);
  SB(); GM();
  WAITL(4);
  MF32("a", acc[0], wg[18], xf[2]);
  DSR128(xf[7], xadr[1], 8192);
  SB(); GM();
  WAITL(4);
  MF32("a", acc[0], wg[19], xf[3]);
  DSR128(xf[0], xadr[1], 12288);
  SB(); GM();
  WAITL(4);
  MF32("a", acc[0], wg[20], xf[4]);
  DSR128(xf[1], xadr[1], 16384);
  { const unsigned so_ = sHp + 917504u; BLOAD2(pollh, v_ph, so_); }
  SB(); GM();
  WAITL(4);
  MF32("a", acc[0], wg[21], xf[5]);
  DSR128(xf[2], xadr[1], 20480);
  asm volatile("v_add_u32 %0, %1, %2" : "=v"(xadr[0]) : "s"(sXg[0] + 28672u), "v"(xl1));
  SB(); GM();
  WAITL(4);
  MF32("a", acc[0], wg[22], xf[6]);
  DSR128(xf[3], xadr[1], 24576);
  WAITV(4);
  { const unsigned so_ = sHf + 1261568u; BSTORE2(tagv, v_one, so_); }
  SB(); GM();
  WAITL(4);
  MF32("a", acc[0], wg[23], xf[7]);
  SBAR();
  DSR128(xf[4], xadr[0], 0);
  WAITV(3);
  { const unsigned so_ = sXp + 344064u; CHECKF(pollx, v_px, so_); }
  GLDS1(114688, sX3, 1376256);
  SB(); GM();
  WAITL(4);
  MF32("a", acc[0], wg[24], xf[0]);
  DSR128(xf[5], xadr[0], 4096);
  GLDS1(115712, sX3, 1377280);
  SB(); GM();
  WAITL(4);
  MF32("a", acc[0], wg[25], xf[1]);
  DSR128(xf[6], xadr[0], 8192);
  { const unsigned so_ = sGl + 2 * GR_KIND + 229376u; CHECKG(gC, 252, 253, 255, so_); }
  SB(); GM();
  WAITL(4);
  MF32("a", acc[0], wg[26], xf[2]);
  DSR128(xf[7], xadr[0], 12288);
  GLDS1(116736, sX3, 1378304);
  SB(); GM();
  WAITL(4);
  MF32("a", acc[0], wg[27], xf[3]);
  DSR128(xf[0], xadr[0], 16384);
  asm volatile("v_add_f32_dpp %0, %1, %1 row_ror:2 row_mask:0xf bank_mask:0xf\n\ts_nop 1\n\tv_add_f32_dpp %0, %0, %0 row_ror:4 row_mask:0xf bank_mask:0xf\n\ts_nop 1\n\tv_add_f32_dpp %0, %0, %0 row_ror:8 row_mask:0xf bank_mask:0xf" : "=&v"(kp) : "v"(gC[0]));
  SB(); GM();
  WAITL(4);
  MF32Z("v", acc[1], wg[0], xf[4]);
  DSR128(xf[1], xadr[0], 20480);
  asm volatile("v_add_u32 %0, %1, %2" : "=v"(xadr[1]) : "s"(sXg[1] + 28672u), "v"(xl1));
  GLDS1(117760, sX3, 1379328);
  SB(); GM();
  WAITL(4);
  MF32("v", acc[1], wg[1], xf[5]);
  DSR128(xf[2], xadr[0], 24576);
  SBAR();
  GLDS1(0, sX3, 1605632);
  SB(); GM();
  WAITL(4);
  MF32("v", acc[1], wg[2], xf[6]);
  DSR128(xf[3], xadr[1], 0);
  GLDS1(118784, sX3, 1380352);
  SB(); GM();
  WAITL(4);
  MF32("v", acc[1], wg[3], xf[7]);
  DSR128(xf[4], xadr[1], 4096);
  asm volatile("v_max_f32 %0, 0, %2\n\tv_max_f32 %1, 0, %3\n\tv_mul_f32 %0, %0, %4\n\tv_mul_f32 %1, %1, %5" : "=&v"(ep0), "=&v"(ep1) : "v"(acc[0][0]), "v"(acc[0][1]), "v"(acc[0][8]), "v"(acc[0][9]));
  SB(); GM();
  WAITL(4);
  MF32("v", acc[1], wg[4], xf[0]);
  DSR128(xf[5], xadr[1], 8192);
  GLDS1(119808, sX3, 1381376);
  asm volatile("v_cvt_pk_bf16_f32 %0, %1, %2" : "=v"(hp0) : "v"(ep0), "v"(ep1));
  SB(); GM();
  WAITL(4);
  MF32("v", acc[1], wg[5], xf[1]);
  DSR128(xf[6], xadr[1], 12288);
  WAITV(8);
  { const unsigned so_ = sHp + 917504u; CHECKF(pollh, v_ph, so_); }
  asm volatile("v_max_f32 %0, 0, %1" : "=v"(ep0) : "v"(acc[0][2]));
  SB(); GM();
  WAITL(4);
  MF32("a", acc[1], wg[6], xf[2]);
  DSR128(xf[7], xadr[1], 16384);
  GLDS1(120832, sX3, 1382400);
  asm volatile("v_max_f32 %0, 0, %1" : "=v"(ep1) : "v"(acc[0][3]));
  SB(); GM();
  WAITL(4);
  MF32("a", acc[1], wg[7], xf[3]);
  DSR128(xf[0], xadr[1], 20480);
  asm volatile("v_add_u32 %0, %1, %2" : "=v"(xadr[0]) : "s"(sXg[2] + 28672u), "v"(xl1));
  GLDS1(1024, sX3, 1606656);
  SB(); GM();
  WAITL(4);
  MF32("a", acc[1], wg[8], xf[4]);
  DSR128(xf[1], xadr[1], 24576);
  asm volatile("v_mul_f32 %0, %0, %3\n\tv_mul_f32 %1, %1, %4\n\tv_cvt_pk_bf16_f32 %2, %0, %1\n\tv_max_f32 %0, 0, %5" : "+v"(ep0), "+v"(ep1), "=&v"(hp1) : "v"(acc[0][10]), "v"(acc[0][11]), "v"(acc[0][4]));
  SB(); GM();
  WAITL(4);
  MF32("a", acc[1], wg[9], xf[5]);
  DSR128(xf[2], xadr[0], 0);
  GLDS1(2048, sX3, 1607680);
  asm volatile("v_max_f32 %0, 0, %1" : "=v"(ep1) : "v"(acc[0][5]));
  SB(); GM();
  WAITL(4);
  MF32("a", acc[1], wg[10], xf[6]);
  DSR128(xf[3], xadr[0], 4096);
  asm volatile("v_mul_f32 %0, %0, %3\n\tv_mul_f32 %1, %1, %4\n\tv_cvt_pk_bf16_f32 %2, %0, %1\n\tv_max_f32 %0, 0, %5" : "+v"(ep0), "+v"(ep1), "=&v"(hp2) : "v"(acc[0][12]), "v"(acc[0][13]), "v"(acc[0][6]));
  SB(); GM();
  WAITL(4);
  MF32("a", acc[1], wg[11], xf[7]);
  DSR128(xf[4], xadr[0], 8192);
  GLDS1(3072, sX3, 1608704);
  asm volatile("v_max_f32 %0, 0, %1" : "=v"(ep1) : "v"(acc[0][7]));
  SB(); GM();
  WAITL(4);
  MF32("a", acc[1], wg[12], xf[0]);
  DSR128(xf[5], xadr[0], 12288);
  asm volatile("v_mul_f32 %0, %0, %3\n\tv_mul_f32 %1, %1, %4\n\tv_cvt_pk_bf16_f32 %2, %0, %1" : "+v"(ep0), "+v"(ep1), "=&v"(hp3) : "v"(acc[0][14]), "v"(acc[0][15]));
  SB(); GM();
  WAITL(4);
  MF32("a", acc[1], wg[13], xf[1]);
  DSR128(xf[6], xadr[0], 16384);
  GLDS1(4096, sX3, 1609728);
  SB(); GM();
  WAITL(4);
  MF32("a", acc[1], wg[14], xf[2]);
  DSR128(xf[7], xadr[0], 20480);
  asm volatile("v_add_u32 %0, %1, %2" : "=v"(xadr[1]) : "s"(sXg[3] + 28672u), "v"(xl1));
  { hfr = v4u{hp0, hp1, hp2, hp3}; const unsigned so_ = sHst + 1 * H_LAYER + 3670016u; BSTORE4(hfr, v_hst, so_); }
  SB(); GM();
  WAITL(4);
  MF32("a", acc[1], wg[15], xf[3]);
  DSR128(xf[0], xadr[0], 24576);
  asm volatile("v_mul_f32 %0, %1, %2\n\tv_fma_f32 %0, -%3, %3, %0\n\tv_max_f32 %0, 0, %0" : "=&v"(kv) : "v"(kp), "v"(vinvD), "v"(dm2));
  SB(); GM();
  WAITL(4);
  MF32("a", acc[1], wg[16], xf[4]);
  DSR128(xf[1], xadr[1], 0);
  GLDS1(5120, sX3, 1610752);
  SB(); GM();
  WAITL(4);
  MF32("a", acc[1], wg[17], xf[5]);
  DSR128(xf[2], xadr[1], 4096);
  asm volatile("v_cvt_pk_bf16_f32 %0, %1, %1\n\tv_lshlrev_b32 %1, 16, %0\n\tv_add_f32 %1, %2, %1" : "=&v"(kt), "+v"(kv) : "s"(sEps));
  SB(); GM();
  WAITL(4);
  MF32("a", acc[1], wg[18], xf[6]);
  DSR128(xf[3], xadr[1], 8192);
  GLDS1(6144, sX3, 1611776);
  SB(); GM();
  WAITL(4);
  MF32("a", acc[1], wg[19], xf[7]);
  DSR128(xf[4], xadr[1], 12288);
  asm volatile("v_cvt_pk_bf16_f32 %0, %1, %1\n\tv_lshlrev_b32 %1, 16, %0" : "=&v"(kt), "+v"(kv) : );
  SB(); GM();
  WAITL(4);
  MF32("a", acc[1], wg[20], xf[0]);
  DSR128(xf[5], xadr[1], 16384);
  { const unsigned so_ = sHl + 1 * H_LAYER + 0u; BLOAD4(H[0], v_l16, so_, 0); }
  SB(); GM();
  WAITL(4);
  MF32("a", acc[1], wg[21], xf[1]);
  DSR128(xf[6], xadr[1], 20480);
  asm volatile("v_add_u32 %0, %1, %2" : "=v"(xadr[0]) : "s"(sXg[0] + 114688u), "v"(xl1));
  asm volatile("v_sqrt_f32 %0, %0" : "+v"(kv) : );
  SB(); GM();
  WAITL(4);
  MF32("a", acc[1], wg[22], xf[2]);
  DSR128(xf[7], xadr[1], 24576);
  { const unsigned so_ = sHl + 1 * H_LAYER + 0u; BLOAD4(H[1], v_l16, so_, 1024); }
  asm volatile("v_cvt_pk_bf16_f32 %0, %1, %1\n\tv_lshlrev_b32 %1, 16, %0" : "=&v"(kt), "+v"(kv) : );
  SB(); GM();
  WAITL(4);
  MF32("a", acc[1], wg[23], xf[3]);
  asm volatile("v_rcp_f32 %0, %0\n\ts_nop 0\n\tv_mul_f32 %1, %0, %0" : "+v"(kv), "=&v"(rsq6) : );
  SB(); STUB(15, ".Lwret15"); GM();
  WAITL(3);
  MF32("a", acc[1], wg[24], xf[4]);
  { const unsigned so_ = sHl + 1 * H_LAYER + 0u; BLOAD4(H[2], v_l16, so_, 2048); }
  SB(); GM();
  WAITL(2);
  MF32("a", acc[1], wg[25], xf[5]);
  SB(); GM();
  WAITL(1);
  MF32("a", acc[1], wg[26], xf[6]);
  SB(); GM();
  WAITL(0);
  MF32("a", acc[1], wg[27], xf[7]);
  { const unsigned so_ = sHl + 1 * H_LAYER + 0u; BLOAD4(H[3], v_l16, so_, 3072); }
  SB(); GM();
  { const unsigned so_ = sHl + 1 * H_LAYER + 4096u; BLOAD4(H[4], v_l16, so_, 0); }
  SB(); GM();
  { const unsigned so_ = sHl + 1 * H_LAYER + 4096u; BLOAD4(H[5], v_l16, so_, 1024); }
  SB(); GM();
  asm volatile("s_nop 7");
  asm volatile("s_nop 7");
  asm volatile("v_max_f32 %0, 0, %3\n\tv_max_f32 %1, 0, %4\n\tv_mul_f32 %0, %0, %5\n\tv_mul_f32 %1, %1, %6\n\tv_cvt_pk_bf16_f32 %2, %0, %1\n\tv_max_f32 %0, 0, %7\n\tv_max_f32 %1, 0, %8" : "=&v"(ep0), "=&v"(ep1), "=&v"(hp0) : "v"(acc[1][0]), "v"(acc[1][1]), "v"(acc[1][8]), "v"(acc[1][9]), "v"(acc[1][2]), "v"(acc[1][3]));
  asm volatile("v_mul_f32 %0, %0, %3\n\tv_mul_f32 %1, %1, %4\n\tv_cvt_pk_bf16_f32 %2, %0, %1\n\tv_max_f32 %0, 0, %5" : "+v"(ep0), "+v"(ep1), "=&v"(hp1) : "v"(acc[1][10]), "v"(acc[1][11]), "v"(acc[1][4]));
  SB(); GM();
  asm volatile("v_max_f32 %0, 0, %3\n\tv_mul_f32 %1, %1, %4\n\tv_mul_f32 %0, %0, %5\n\tv_cvt_pk_bf16_f32 %2, %1, %0\n\tv_max_f32 %1, 0, %6\n\tv_max_f32 %0, 0, %7\n\tv_mul_f32 %1, %1, %8" : "=&v"(ep1), "+v"(ep0), "=&v"(hp2) : "v"(acc[1][5]), "v"(acc[1][12]), "v"(acc[1][13]), "v"(acc[1][6]), "v"(acc[1][7]), "v"(acc[1][14]));
  asm volatile("v_mul_f32 %0, %0, %2\n\tv_cvt_pk_bf16_f32 %1, %3, %0" : "+v"(ep1), "=&v"(hp3) : "v"(acc[1][15]), "v"(ep0));
  SB(); GM();
  { const unsigned so_ = sHl + 1 * H_LAYER + 4096u; BLOAD4(H[6], v_l16, so_, 2048); }
  { hfr = v4u{hp0, hp1, hp2, hp3}; const unsigned so_ = sHst + 1 * H_LAYER + 4587520u; BSTORE4(hfr, v_hst, so_); }
  SB(); GM();
  { const unsigned so_ = sHl + 1 * H_LAYER + 4096u; BLOAD4(H[7], v_l16, so_, 3072); }
  SB(); GM();
  WAITV(16);
  SBAR();
  DSR128(xf[0], xadr[0], 0);
  DSR128(xf[1], xadr[0], 4096);
  DSR128(xf[2], xadr[0], 8192);
  DSR128(xf[3], xadr[0], 12288);
  DSR128(xf[4], xadr[0], 16384);
  WAITL(4);
  MF32Z("v", acc[0], wg[0], xf[0]);
  DSR128(xf[5], xadr[0], 20480);
  asm volatile("v_add_u32 %0, %1, %2" : "=v"(xadr[1]) : "s"(sXg[1] + 114688u), "v"(xl1));
  { const unsigned so_ = sHl + 1 * H_LAYER + 8192u; BLOAD4(H[8], v_l16, so_, 0); }
  SB(); GM();
  WAITL(4);
  MF32("v", acc[0], wg[1], xf[1]);
  DSR128(xf[6], xadr[0], 24576);
  SB(); GM();
  WAITL(4);
  MF32("v", acc[0], wg[2], xf[2]);
  DSR128(xf[7], xadr[1], 0);
  { const unsigned so_ = sHl + 1 * H_LAYER + 8192u; BLOAD4(H[9], v_l16, so_, 1024); }
  SB(); GM();
  WAITL(4);
  MF32("v", acc[0], wg[3], xf[3]);
  DSR128(xf[0], xadr[1], 4096);
  SB(); GM();
  WAITL(4);
  MF32("v", acc[0], wg[4], xf[4]);
  DSR128(xf[1], xadr[1], 8192);
  { const unsigned so_ = sHl + 1 * H_LAYER + 8192u; BLOAD4(H[10], v_l16, so_, 2048); }
  SB(); GM();
  WAITL(4);
  MF32("v", acc[0], wg[5], xf[5]);
  DSR128(xf[2], xadr[1], 12288);
  SB(); GM();
  WAITL(4);
  MF32("a", acc[0], wg[6], xf[6]);
  DSR128(xf[3], xadr[1], 16384);
  { const unsigned so_ = sHl + 1 * H_LAYER + 8192u; BLOAD4(H[11], v_l16, so_, 3072); }
  SB(); GM();
  WAITL(4);
  MF32("a", acc[0], wg[7], xf[7]);
  DSR128(xf[4], xadr[1], 20480);
  asm volatile("v_add_u32 %0, %1, %2" : "=v"(xadr[0]) : "s"(sXg[2] + 114688u), "v"(xl1));
  WAITV(15);
  { const unsigned so_ = sHf + 1376256u; BSTORE2(tagv, v_one, so_); }
  SB(); GM();
  WAITL(4);
  MF32("a", acc[0], wg[8], xf[0]);
  DSR128(xf[5], xadr[1], 24576);
  { const unsigned so_ = sHl + 1 * H_LAYER + 12288u; BLOAD4(H[12], v_l16, so_, 0); }
  SB(); GM();
  WAITL(4);
  MF32("a", acc[0], wg[9], xf[1]);
  DSR128(xf[6], xadr[0], 0);
  SB(); GM();
  WAITL(4);
  MF32("a", acc[0], wg[10], xf[2]);
  DSR128(xf[7], xadr[0], 4096);
  { const unsigned so_ = sHl + 1 * H_LAYER + 12288u; BLOAD4(H[13], v_l16, so_, 1024); }
  { const unsigned so_ = sHp + 1146880u; BLOAD2(pollh, v_ph, so_); }
  SB(); GM();
  WAITL(4);
  MF32("a", acc[0], wg[11], xf[3]);
  DSR128(xf[0], xadr[0], 8192);
  SB(); GM();
  WAITL(4);
  MF32("a", acc[0], wg[12], xf[4]);
  DSR128(xf[1], xadr[0], 12288);
  SB(); GM();
  WAITL(4);
  MF32("a", acc[0], wg[13], xf[5]);
  DSR128(xf[2], xadr[0], 16384);
  SB(); GM();
  WAITL(4);
  MF32("a", acc[0], wg[14], xf[6]);
  DSR128(xf[3], xadr[0], 20480);
  asm volatile("v_add_u32 %0, %1, %2" : "=v"(xadr[1]) : "s"(sXg[3] + 114688u), "v"(xl1));
  SB(); GM();
  WAITL(4);
  MF32("a", acc[0], wg[15], xf[7]);
  DSR128(xf[4], xadr[0], 24576);
  SB(); GM();
  WAITL(4);
  MF32("a", acc[0], wg[16], xf[0]);
  DSR128(xf[5], xadr[1], 0);
  SB(); GM();
  WAITL(4);
  MF32("a", acc[0], wg[17], xf[1]);
  DSR128(xf[6], xadr[1], 4096);
  SB(); GM();
  WAITL(4);
  MF32("a", acc[0], wg[18], xf[2]);
  DSR128(xf[7], xadr[1], 8192);
  SB(); GM();
  WAITL(4);
  MF32("a", acc[0], wg[19], xf[3]);
  DSR128(xf[0], xadr[1], 12288);
  SB(); GM();
  WAITL(4);
  MF32("a", acc[0], wg[20], xf[4]);
  DSR128(xf[1], xadr[1], 16384);
  SB(); GM();
  WAITL(4);
  MF32("a", acc[0], wg[21], xf[5]);
  DSR128(xf[2], xadr[1], 20480);
  asm volatile("v_add_u32 %0, %1, %2" : "=v"(xadr[0]) : "s"(sXg[0] + 0u), "v"(xl1));
  SB(); GM();
  WAITL(4);
  MF32("a", acc[0], wg[22], xf[6]);
  DSR128(xf[3], xadr[1], 24576);
  { const unsigned so_ = sGl + 2 * GR_KIND + 344064u; GLOADG(gC, 252, 255, so_); }
  SB(); GM();
  WAITL(4);
  MF32("a", acc[0], wg[23], xf[7]);
  WAITV(18);
  SBAR();
  DSR128(xf[4], xadr[0], 0);
  SB(); GM();
  WAITL(4);
  MF32("a", acc[0], wg[24], xf[0]);
  DSR128(xf[5], xadr[0], 4096);
  SB(); GM();
  WAITL(4);
  MF32("a", acc[0], wg[25], xf[1]);
  DSR128(xf[6], xadr[0], 8192);
  SB(); GM();
  WAITL(4);
  MF32("a", acc[0], wg[26], xf[2]);
  DSR128(xf[7], xadr[0], 12288);
  SB(); GM();
  WAITL(4);
  MF32("a", acc[0], wg[27], xf[3]);
  DSR128(xf[0], xadr[0], 16384);
  WAITV(10);
  { const unsigned so_ = sHf + 1490944u; BSTORE2(tagv, v_one, so_); }
  SB(); GM();
  WAITL(4);
  MF32Z("v", acc[1], wg[0], xf[4]);
  DSR128(xf[1], xadr[0], 20480);
  asm volatile("v_add_u32 %0, %1, %2" : "=v"(xadr[1]) : "s"(sXg[1] + 0u), "v"(xl1));
  SB(); GM();
  WAITL(4);
  MF32("v", acc[1], wg[1], xf[5]);
  DSR128(xf[2], xadr[0], 24576);
  SB(); GM();
  WAITL(4);
  MF32("v", acc[1], wg[2], xf[6]);
  DSR128(xf[3], xadr[1], 0);
  SB(); GM();
  WAITL(4);
  MF32("v", acc[1], wg[3], xf[7]);
  DSR128(xf[4], xadr[1], 4096);
  asm volatile("v_max_f32 %0, 0, %2\n\tv_max_f32 %1, 0, %3\n\tv_mul_f32 %0, %0, %4\n\tv_mul_f32 %1, %1, %5" : "=&v"(ep0), "=&v"(ep1) : "v"(acc[0][0]), "v"(acc[0][1]), "v"(acc[0][8]), "v"(acc[0][9]));
  SB(); GM();
  WAITL(4);
  MF32("v", acc[1], wg[4], xf[0]);
  DSR128(xf[5], xadr[1], 8192);
  asm volatile("v_cvt_pk_bf16_f32 %0, %1, %2\n\tv_max_f32 %1, 0, %3\n\tv_max_f32 %2, 0, %4\n\tv_mul_f32 %1, %1, %5" : "=&v"(hp0), "+v"(ep0), "+v"(ep1) : "v"(acc[0][2]), "v"(acc[0][3]), "v"(acc[0][10]));
  SB(); GM();
  WAITL(4);
  MF32("v", acc[1], wg[5], xf[1]);
  DSR128(xf[6], xadr[1], 12288);
  asm volatile("v_mul_f32 %0, %0, %3\n\tv_cvt_pk_bf16_f32 %1, %2, %0\n\tv_max_f32 %2, 0, %4\n\tv_max_f32 %0, 0, %5" : "+v"(ep1), "=&v"(hp1), "+v"(ep0) : "v"(acc[0][11]), "v"(acc[0][4]), "v"(acc[0][5]));
  SB(); GM();
  WAITL(4);
  MF32("a", acc[1], wg[6], xf[2]);
  DSR128(xf[7], xadr[1], 16384);
  asm volatile("v_mul_f32 %0, %0, %3\n\tv_mul_f32 %1, %1, %4\n\tv_cvt_pk_bf16_f32 %2, %0, %1\n\tv_max_f32 %0, 0, %5" : "+v"(ep0), "+v"(ep1), "=&v"(hp2) : "v"(acc[0][12]), "v"(acc[0][13]), "v"(acc[0][6]));
  SB(); GM();
  WAITL(4);
  MF32("a", acc[1], wg[7], xf[3]);
  DSR128(xf[0], xadr[1], 20480);
  asm volatile("v_add_u32 %0, %1, %2" : "=v"(xadr[0]) : "s"(sXg[2] + 0u), "v"(xl1));
  asm volatile("v_max_f32 %0, 0, %2\n\tv_mul_f32 %1, %1, %3\n\tv_mul_f32 %0, %0, %4" : "=&v"(ep1), "+v"(ep0) : "v"(acc[0][7]), "v"(acc[0][14]), "v"(acc[0][15]));
  SB(); GM();
  WAITL(4);
  MF32("a", acc[1], wg[8], xf[4]);
  DSR128(xf[1], xadr[1], 24576);
  asm volatile("v_cvt_pk_bf16_f32 %0, %1, %2" : "=v"(hp3) : "v"(ep0), "v"(ep1));
  { hfr = v4u{hp0, hp1, hp2, hp3}; const unsigned so_ = sHst + 1 * H_LAYER + 5505024u; BSTORE4(hfr, v_hst, so_); }
  SB(); GM();
  WAITL(4);
  MF32("a", acc[1], wg[9], xf[5]);
  DSR128(xf[2], xadr[0], 0);
  SB(); GM();
  WAITL(4);
  MF32("a", acc[1], wg[10], xf[6]);
  DSR128(xf[3], xadr[0], 4096);
  SB(); GM();
  WAITL(4);
  MF32("a", acc[1], wg[11], xf[7]);
  DSR128(xf[4], xadr[0], 8192);
  SB(); GM();
  WAITL(4);
  MF32("a", acc[1], wg[12], xf[0]);
  DSR128(xf[5], xadr[0], 12288);
  SB(); GM();
  WAITL(4);
  MF32("a", acc[1], wg[13], xf[1]);
  DSR128(xf[6], xadr[0], 16384);
  SB(); GM();
  WAITL(4);
  MF32("a", acc[1], wg[14], xf[2]);
  DSR128(xf[7], xadr[0], 20480);
  asm volatile("v_add_u32 %0, %1, %2" : "=v"(xadr[1]) : "s"(sXg[3] + 0u), "v"(xl1));
  WAITV(3);
  { const unsigned so_ = sHp + 1146880u; CHECKF(pollh, v_ph, so_); }
  SB(); GM();
  WAITL(4);
  MF32("a", acc[1], wg[15], xf[3]);
  DSR128(xf[0], xadr[0], 24576);
  WAITV(2);
  { const unsigned so_ = sGl + 2 * GR_KIND + 344064u; CHECKG(gC, 252, 253, 255, so_); }
  SB(); GM();
  WAITL(4);
  MF32("a", acc[1], wg[16], xf[4]);
  DSR128(xf[1], xadr[1], 0);
  asm volatile("v_add_f32_dpp %0, %1, %1 row_ror:2 row_mask:0xf bank_mask:0xf\n\ts_nop 1\n\tv_add_f32_dpp %0, %0, %0 row_ror:4 row_mask:0xf bank_mask:0xf\n\ts_nop 1\n\tv_add_f32_dpp %0, %0, %0 row_ror:8 row_mask:0xf bank_mask:0xf" : "=&v"(kp) : "v"(gC[0]));
  SB(); GM();
  WAITL(4);
  MF32("a", acc[1], wg[17], xf[5]);
  DSR128(xf[2], xadr[1], 4096);
  asm volatile("v_mul_f32 %0, %1, %2\n\tv_fma_f32 %0, -%3, %3, %0\n\tv_max_f32 %0, 0, %0" : "=&v"(kv) : "v"(kp), "v"(vinvD), "v"(dm3));
  SB(); GM();
  WAITL(4);
  MF32("a", acc[1], wg[18], xf[6]);
  DSR128(xf[3], xadr[1], 8192);
  asm volatile("v_cvt_pk_bf16_f32 %0, %1, %1\n\tv_lshlrev_b32 %1, 16, %0\n\tv_add_f32 %1, %2, %1" : "=&v"(kt), "+v"(kv) : "s"(sEps));
  SB(); GM();
  WAITL(4);
  MF32("a", acc[1], wg[19], xf[7]);
  DSR128(xf[4], xadr[1], 12288);
  asm volatile("v_cvt_pk_bf16_f32 %0, %1, %1\n\tv_lshlrev_b32 %1, 16, %0" : "=&v"(kt), "+v"(kv) : );
  SB(); GM();
  WAITL(4);
  MF32("a", acc[1], wg[20], xf[0]);
  DSR128(xf[5], xadr[1], 16384);
  asm volatile("v_sqrt_f32 %0, %0\n\ts_nop 0\n\tv_cvt_pk_bf16_f32 %1, %0, %0" : "+v"(kv), "=&v"(kt) : );
  SB(); GM();
  WAITL(4);
  MF32("a", acc[1], wg[21], xf[1]);
  DSR128(xf[6], xadr[1], 20480);
  asm volatile("v_lshlrev_b32 %0, 16, %1\n\tv_rcp_f32 %0, %0" : "=&v"(kv) : "v"(kt));
  SB(); GM();
  WAITL(4);
  MF32("a", acc[1], wg[22], xf[2]);
  DSR128(xf[7], xadr[1], 24576);
  asm volatile("v_mul_f32 %0, %1, %1" : "=v"(rsq7) : "v"(kv));
  SB(); GM();
  WAITL(4);
  MF32("a", acc[1], wg[23], xf[3]);
  DSR128(xf[0], wd_rd, 0);
  SB(); GM();
  WAITL(4);
  MF32("a", acc[1], wg[24], xf[4]);
  DSR128(xf[1], wd_rd, 1024);
  WAITV(0);
  { const unsigned so_ = sHf + 1605632u; BSTORE2(tagv, v_one, so_); }
  SB(); GM();
  WAITL(4);
  MF32("a", acc[1], wg[25], xf[5]);
  DSR128(xf[2], wd_rd, 2048);
  SB(); GM();
  WAITL(4);
  MF32("a", acc[1], wg[26], xf[6]);
  DSR128(xf[3], wd_rd, 3072);
  SB(); GM();
  WAITL(4);
  MF32("a", acc[1], wg[27], xf[7]);
  DSR128(xf[4], wd_rd, 4096);
  SB(); GM();
  MF16Z("a", acc2[0][0], wd[0][0], H[0]);
  asm volatile("v_lshlrev_b32 %0, 16, %2\n\tv_and_b32 %1, 0xffff0000, %2" : "=&v"(rf0), "=&v"(rf1) : "v"(x3p0_0));
  SB(); GM();
  MF16Z("a", acc2[0][1], wd[0][1], H[0]);
  asm volatile("v_lshlrev_b32 %0, 16, %2\n\tv_and_b32 %1, 0xffff0000, %2" : "=&v"(rf2), "=&v"(rf3) : "v"(x3p0_1));
  SB(); GM();
  MF16Z("a", acc2[0][2], wd[0][2], H[0]);
  asm volatile("v_xor_b32 %0, 64, %2\n\tv_xor_b32 %1, 128, %2" : "=&v"(rw1), "=&v"(rw2) : "v"(red_wr));
  SB(); GM();
  WAITL(4);
  MF16Z("v", acc2[0][3], xf[0], H[0]);
  { const unsigned so_ = sHl + 1 * H_LAYER + 1835008u; BLOAD4(H[0], v_l16, so_, 0); }
  SB(); GM();
  MF16("a", acc2[0][0], wd[1][0], H[1]);
  asm volatile("v_xor_b32 %0, 192, %1" : "=v"(rw3) : "v"(red_wr));
  SB(); GM();
  MF16("a", acc2[0][1], wd[1][1], H[1]);
  asm volatile("v_max_f32 %0, 0, %3\n\tv_max_f32 %1, 0, %4\n\tv_mul_f32 %0, %0, %5\n\tv_mul_f32 %1, %1, %6\n\tv_cvt_pk_bf16_f32 %2, %0, %1" : "=&v"(ep0), "=&v"(ep1), "=&v"(hp0) : "v"(acc[1][0]), "v"(acc[1][1]), "v"(acc[1][8]), "v"(acc[1][9]));
  SB(); GM();
  MF16("a", acc2[0][2], wd[1][2], H[1]);
  asm volatile("v_max_f32 %0, 0, %3\n\tv_max_f32 %1, 0, %4\n\tv_mul_f32 %0, %0, %5\n\tv_mul_f32 %1, %1, %6\n\tv_cvt_pk_bf16_f32 %2, %0, %1" : "=&v"(ep0), "=&v"(ep1), "=&v"(hp1) : "v"(acc[1][2]), "v"(acc[1][3]), "v"(acc[1][10]), "v"(acc[1][11]));
  SB(); GM();
  WAITL(3);
  MF16("v", acc2[0][3], xf[1], H[1]);
  { const unsigned so_ = sHl + 1 * H_LAYER + 1835008u; BLOAD4(H[1], v_l16, so_, 1024); }
  asm volatile("v_max_f32 %0, 0, %2\n\tv_max_f32 %1, 0, %3\n\tv_mul_f32 %0, %0, %4" : "=&v"(ep0), "=&v"(ep1) : "v"(acc[1][4]), "v"(acc[1][5]), "v"(acc[1][12]));
  SB(); GM();
  MF16("a", acc2[0][0], wd[2][0], H[2]);
  asm volatile("v_mul_f32 %0, %0, %3\n\tv_cvt_pk_bf16_f32 %1, %2, %0\n\tv_max_f32 %2, 0, %4\n\tv_max_f32 %0, 0, %5\n\tv_mul_f32 %2, %2, %6" : "+v"(ep1), "=&v"(hp2), "+v"(ep0) : "v"(acc[1][13]), "v"(acc[1][6]), "v"(acc[1][7]), "v"(acc[1][14]));
  SB(); GM();
  MF16("a", acc2[0][1], wd[2][1], H[2]);
  asm volatile("v_mul_f32 %0, %0, %2\n\tv_cvt_pk_bf16_f32 %1, %3, %0" : "+v"(ep1), "=&v"(hp3) : "v"(acc[1][15]), "v"(ep0));
  { hfr = v4u{hp0, hp1, hp2, hp3}; const unsigned so_ = sHst + 1 * H_LAYER + 6422528u; BSTORE4(hfr, v_hst, so_); }
  SB(); GM();
  MF16("a", acc2[0][2], wd[2][2], H[2]);
  SB(); GM();
  WAITL(2);
  MF16("v", acc2[0][3], xf[2], H[2]);
  { const unsigned so_ = sHl + 1 * H_LAYER + 1835008u; BLOAD4(H[2], v_l16, so_, 2048); }
  SB(); GM();
  MF16("a", acc2[0][0], wd[3][0], H[3]);
  SB(); GM();
  MF16("a", acc2[0][1], wd[3][1], H[3]);
  DSR128(xf[5], wd_rd, 5120);
  SB(); GM();
  MF16("a", acc2[0][2], wd[3][2], H[3]);
  SB(); GM();
  WAITL(2);
  MF16("v", acc2[0][3], xf[3], H[3]);
  { const unsigned so_ = sHl + 1 * H_LAYER + 1835008u; BLOAD4(H[3], v_l16, so_, 3072); }
  SB(); GM();
  MF16("a", acc2[0][0], wd[4][0], H[4]);
  SB(); GM();
  MF16("a", acc2[0][1], wd[4][1], H[4]);
  DSR128(xf[6], wd_rd, 6144);
  SB(); GM();
  MF16("a", acc2[0][2], wd[4][2], H[4]);
  SB(); GM();
  WAITL(2);
  MF16("v", acc2[0][3], xf[4], H[4]);
  { const unsigned so_ = sHl + 1 * H_LAYER + 1839104u; BLOAD4(H[4], v_l16, so_, 0); }
  SB(); GM();
  MF16("a", acc2[0][0], wd[5][0], H[5]);
  SB(); GM();
  MF16("a", acc2[0][1], wd[5][1], H[5]);
  DSR128(xf[7], wd_rd, 7168);
  SB(); GM();
  MF16("a", acc2[0][2], wd[5][2], H[5]);
  SB(); GM();
  WAITL(2);
  MF16("v", acc2[0][3], xf[5], H[5]);
  { const unsigned so_ = sHl + 1 * H_LAYER + 1839104u; BLOAD4(H[5], v_l16, so_, 1024); }
  SB(); GM();
  MF16("a", acc2[0][0], wd[6][0], H[6]);
  SB(); GM();
  MF16("a", acc2[0][1], wd[6][1], H[6]);
  DSR128(xf[0], wd_rd, 8192);
  SB(); GM();
  MF16("a", acc2[0][2], wd[6][2], H[6]);
  SB(); GM();
  WAITL(2);
  MF16("v", acc2[0][3], xf[6], H[6]);
  { const unsigned so_ = sHl + 1 * H_LAYER + 1839104u; BLOAD4(H[6], v_l16, so_, 2048); }
  SB(); GM();
  MF16("a", acc2[0][0], wd[7][0], H[7]);
  SB(); GM();
  MF16("a", acc2[0][1], wd[7][1], H[7]);
  DSR128(xf[1], wd_rd, 9216);
  SB(); GM();
  MF16("a", acc2[0][2], wd[7][2], H[7]);
  SB(); GM();
  WAITL(2);
  MF16("v", acc2[0][3], xf[7], H[7]);
  { const unsigned so_ = sHl + 1 * H_LAYER + 1839104u; BLOAD4(H[7], v_l16, so_, 3072); }
  SB(); GM();
  MF16("a", acc2[0][0], wd[8][0], H[8]);
  SB(); GM();
  MF16("a", acc2[0][1], wd[8][1], H[8]);
  DSR128(xf[2], wd_rd, 10240);
  SB(); GM();
  MF16("a", acc2[0][2], wd[8][2], H[8]);
  SB(); GM();
  WAITL(2);
  MF16("v", acc2[0][3], xf[0], H[8]);
  { const unsigned so_ = sHl + 1 * H_LAYER + 1843200u; BLOAD4(H[8], v_l16, so_, 0); }
  SB(); GM();
  MF16("a", acc2[0][0], wd[9][0], H[9]);
  SB(); GM();
  MF16("a", acc2[0][1], wd[9][1], H[9]);
  DSR128(xf[3], wd_rd, 11264);
  SB(); GM();
  MF16("a", acc2[0][2], wd[9][2], H[9]);
  SB(); GM();
  WAITL(2);
  MF16("v", acc2[0][3], xf[1], H[9]);
  { const unsigned so_ = sHl + 1 * H_LAYER + 1843200u; BLOAD4(H[9], v_l16, so_, 1024); }
  SB(); GM();
  MF16("a", acc2[0][0], wd[10][0], H[10]);
  SB(); GM();
  MF16("a", acc2[0][1], wd[10][1], H[10]);
  DSR128(xf[4], wd_rd, 12288);
  SB(); GM();
  MF16("a", acc2[0][2], wd[10][2], H[10]);
  SB(); GM();
  WAITL(2);
  MF16("v", acc2[0][3], xf[2], H[10]);
  { const unsigned so_ = sHl + 1 * H_LAYER + 1843200u; BLOAD4(H[10], v_l16, so_, 2048); }
  SB(); GM();
  MF16("a", acc2[0][0], wd[11][0], H[11]);
  SB(); GM();
  MF16("a", acc2[0][1], wd[11][1], H[11]);
  DSR128(xf[5], wd_rd, 13312);
  SB(); GM();
  MF16("a", acc2[0][2], wd[11][2], H[11]);
  SB(); GM();
  WAITL(2);
  MF16("v", acc2[0][3], xf[3], H[11]);
  { const unsigned so_ = sHl + 1 * H_LAYER + 1843200u; BLOAD4(H[11], v_l16, so_, 3072); }
  { const unsigned so_ = sHp + 1376256u; BLOAD2(pollh, v_ph, so_); }
  SB(); GM();
  MF16("a", acc2[0][0], wd[12][0], H[12]);
  SB(); GM();
  MF16("a", acc2[0][1], wd[12][1], H[12]);
  DSR128(xf[6], wd_rd, 0);
  SB(); GM();
  MF16("a", acc2[0][2], wd[12][2], H[12]);
  SB(); GM();
  WAITL(2);
  MF16("v", acc2[0][3], xf[4], H[12]);
  { const unsigned so_ = sHl + 1 * H_LAYER + 1847296u; BLOAD4(H[12], v_l16, so_, 0); }
  SB(); GM();
  MF16("a", acc2[0][0], wd[13][0], H[13]);
  SB(); GM();
  MF16("a", acc2[0][1], wd[13][1], H[13]);
  DSR128(xf[7], wd_rd, 1024);
  SB(); GM();
  MF16("a", acc2[0][2], wd[13][2], H[13]);
  SB(); GM();
  WAITL(2);
  MF16("v", acc2[0][3], xf[5], H[13]);
  { const unsigned so_ = sHl + 1 * H_LAYER + 1847296u; BLOAD4(H[13], v_l16, so_, 1024); }
  SB(); GM();
  WAITV(15);
  MF16Z("a", acc2[1][0], wd[0][0], H[0]);
  SB(); GM();
  MF16Z("a", acc2[1][1], wd[0][1], H[0]);
  DSR128(xf[0], wd_rd, 2048);
  SB(); GM();
  MF16Z("a", acc2[1][2], wd[0][2], H[0]);
  SB(); GM();
  WAITL(2);
  MF16Z("v", acc2[1][3], xf[6], H[0]);
  DSW128(red_wr, acc2[0][0], 0);
  SB(); GM();
  WAITV(14);
  MF16("a", acc2[1][0], wd[1][0], H[1]);
  WAITV(13);
  { const unsigned so_ = sHf + 1720320u; BSTORE2(tagv, v_one, so_); }
  SB(); GM();
  MF16("a", acc2[1][1], wd[1][1], H[1]);
  DSR128(xf[1], wd_rd, 3072);
  DSW128(rw1, acc2[0][1], 0);
  SB(); GM();
  MF16("a", acc2[1][2], wd[1][2], H[1]);
  SB(); GM();
  WAITL(4);
  MF16("v", acc2[1][3], xf[7], H[1]);
  DSW128(rw2, acc2[0][2], 0);
  SB(); GM();
  WAITV(13);
  MF16("a", acc2[1][0], wd[2][0], H[2]);
  DSW128(rw3, acc2[0][3], 0);
  SB(); GM();
  MF16("a", acc2[1][1], wd[2][1], H[2]);
  DSR128(xf[2], wd_rd, 4096);
  SB(); GM();
  MF16("a", acc2[1][2], wd[2][2], H[2]);
  SB(); GM();
  WAITL(6);
  MF16("v", acc2[1][3], xf[0], H[2]);
  SB(); GM();
  WAITV(12);
  MF16("a", acc2[1][0], wd[3][0], H[3]);
  SB(); GM();
  MF16("a", acc2[1][1], wd[3][1], H[3]);
  DSR128(xf[3], wd_rd, 5120);
  SB(); GM();
  MF16("a", acc2[1][2], wd[3][2], H[3]);
  SB(); GM();
  WAITL(5);
  MF16("v", acc2[1][3], xf[1], H[3]);
  SB(); GM();
  WAITV(11);
  MF16("a", acc2[1][0], wd[4][0], H[4]);
  WAITL(2);
  SBAR();
  SB(); GM();
  MF16("a", acc2[1][1], wd[4][1], H[4]);
  DSR128(xf[4], wd_rd, 6144);
  DSR128(pr[0], red_rd, 0);
  SB(); GM();
  MF16("a", acc2[1][2], wd[4][2], H[4]);
  DSR128(pr[1], red_rd, 4096);
  DSR128(pr[2], red_rd, 8192);
  SB(); GM();
  WAITL(5);
  MF16("v", acc2[1][3], xf[2], H[4]);
  DSR128(pr[3], red_rd, 12288);
  SB(); GM();
  WAITV(10);
  MF16("a", acc2[1][0], wd[5][0], H[5]);
  SB(); GM();
  MF16("a", acc2[1][1], wd[5][1], H[5]);
  DSR128(xf[5], wd_rd, 7168);
  SB(); GM();
  MF16("a", acc2[1][2], wd[5][2], H[5]);
  SB(); GM();
  WAITL(6);
  MF16("v", acc2[1][3], xf[3], H[5]);
  SB(); GM();
  WAITV(9);
  MF16("a", acc2[1][0], wd[6][0], H[6]);
  WAITL(1);
  SB(); GM();
  MF16("a", acc2[1][1], wd[6][1], H[6]);
  DSR128(xf[6], wd_rd, 8192);
  asm volatile("v_add_f32 %0, %1, %2" : "=v"(fo0) : "v"(pr[0][0]), "v"(pr[1][0]));
  SB(); GM();
  MF16("a", acc2[1][2], wd[6][2], H[6]);
  asm volatile("v_add_f32 %0, %2, %3\n\tv_add_f32 %1, %4, %5" : "=&v"(fo1), "=&v"(fo2) : "v"(pr[0][1]), "v"(pr[1][1]), "v"(pr[0][2]), "v"(pr[1][2]));
  SB(); GM();
  MF16("v", acc2[1][3], xf[4], H[6]);
  asm volatile("v_add_f32 %0, %2, %3\n\tv_add_f32 %1, %1, %4" : "=&v"(fo3), "+v"(fo0) : "v"(pr[0][3]), "v"(pr[1][3]), "v"(pr[2][0]));
  SB(); GM();
  WAITV(8);
  MF16("a", acc2[1][0], wd[7][0], H[7]);
  asm volatile("v_add_f32 %0, %0, %1" : "+v"(fo1) : "v"(pr[2][1]));
  SB(); GM();
  MF16("a", acc2[1][1], wd[7][1], H[7]);
  DSR128(xf[7], wd_rd, 9216);
  asm volatile("v_add_f32 %0, %0, %1" : "+v"(fo2) : "v"(pr[2][2]));
  SB(); GM();
  MF16("a", acc2[1][2], wd[7][2], H[7]);
  asm volatile("v_add_f32 %0, %0, %2\n\tv_add_f32 %1, %1, %3" : "+v"(fo3), "+v"(fo0) : "v"(pr[2][3]), "v"(pr[3][0]));
  SB(); GM();
  WAITL(2);
  MF16("v", acc2[1][3], xf[5], H[7]);
  asm volatile("v_add_f32 %0, %0, %1" : "+v"(fo1) : "v"(pr[3][1]));
  SB(); GM();
  WAITV(7);
  MF16("a", acc2[1][0], wd[8][0], H[8]);
  asm volatile("v_add_f32 %0, %0, %1" : "+v"(fo2) : "v"(pr[3][2]));
  SB(); GM();
  MF16("a", acc2[1][1], wd[8][1], H[8]);
  DSR128(xf[0], wd_rd, 10240);
  asm volatile("v_add_f32 %0, %0, %1" : "+v"(fo3) : "v"(pr[3][3]));
  SB(); GM();
  MF16("a", acc2[1][2], wd[8][2], H[8]);
  asm volatile("v_fma_f32 %0, %0, %2, %3\n\tv_fma_f32 %1, %1, %2, %4" : "+v"(fo0), "+v"(fo1) : "v"(rsq4), "v"(rf0), "v"(rf1));
  SB(); GM();
  WAITL(2);
  MF16("v", acc2[1][3], xf[6], H[8]);
  asm volatile("v_fma_f32 %0, %0, %1, %2" : "+v"(fo2) : "v"(rsq4), "v"(rf2));
  SB(); GM();
  WAITV(6);
  MF16("a", acc2[1][0], wd[9][0], H[9]);
  asm volatile("v_fma_f32 %0, %0, %1, %2" : "+v"(fo3) : "v"(rsq4), "v"(rf3));
  SB(); GM();
  MF16("a", acc2[1][1], wd[9][1], H[9]);
  DSR128(xf[1], wd_rd, 11264);
  asm volatile("v_cvt_pk_bf16_f32 %0, %1, %2" : "=v"(fp0) : "v"(fo0), "v"(fo1));
  SB(); GM();
  MF16("a", acc2[1][2], wd[9][2], H[9]);
  asm volatile("v_cvt_pk_bf16_f32 %0, %1, %2" : "=v"(fp1) : "v"(fo2), "v"(fo3));
  SB(); GM();
  WAITL(2);
  MF16("v", acc2[1][3], xf[7], H[9]);
  { xo = v2u{fp0, fp1}; const unsigned so_ = sOut + 0u; OSTORE2(xo, v_out, so_); }
  SB(); GM();
  WAITV(6);
  MF16("a", acc2[1][0], wd[10][0], H[10]);
  asm volatile("v_lshlrev_b32 %0, 16, %1" : "=v"(rf0) : "v"(x3p1_0));
  SB(); GM();
  MF16("a", acc2[1][1], wd[10][1], H[10]);
  DSR128(xf[2], wd_rd, 12288);
  asm volatile("v_and_b32 %0, 0xffff0000, %1" : "=v"(rf1) : "v"(x3p1_0));
  SB(); GM();
  MF16("a", acc2[1][2], wd[10][2], H[10]);
  asm volatile("v_lshlrev_b32 %0, 16, %2\n\tv_and_b32 %1, 0xffff0000, %2" : "=&v"(rf2), "=&v"(rf3) : "v"(x3p1_1));
  SB(); GM();
  WAITL(2);
  MF16("v", acc2[1][3], xf[0], H[10]);
  asm volatile("v_xor_b32 %0, 64, %1" : "=v"(rw1) : "v"(red_wr));
  SB(); GM();
  WAITV(5);
  MF16("a", acc2[1][0], wd[11][0], H[11]);
  WAITV(4);
  { const unsigned so_ = sHp + 1376256u; CHECKF(pollh, v_ph, so_); }
  { const unsigned so_ = sHl + 1 * H_LAYER + 3670016u; BLOAD4(H[0], v_l16, so_, 0); }
  SB(); GM();
  MF16("a", acc2[1][1], wd[11][1], H[11]);
  DSR128(xf[3], wd_rd, 13312);
  { const unsigned so_ = sHl + 1 * H_LAYER + 3670016u; BLOAD4(H[1], v_l16, so_, 1024); }
  { const unsigned so_ = sHl + 1 * H_LAYER + 3670016u; BLOAD4(H[2], v_l16, so_, 2048); }
  SB(); GM();
  MF16("a", acc2[1][2], wd[11][2], H[11]);
  asm volatile("v_xor_b32 %0, 128, %2\n\tv_xor_b32 %1, 192, %2" : "=&v"(rw2), "=&v"(rw3) : "v"(red_wr));
  SB(); GM();
  WAITL(2);
  MF16("v", acc2[1][3], xf[1], H[11]);
  SBAR();
  SB(); GM();
  WAITV(6);
  MF16("a", acc2[1][0], wd[12][0], H[12]);
  { const unsigned so_ = sHl + 1 * H_LAYER + 3670016u; BLOAD4(H[3], v_l16, so_, 3072); }
  SB(); GM();
  MF16("a", acc2[1][1], wd[12][1], H[12]);
  DSR128(xf[4], wd_rd, 0);
  SB(); GM();
  MF16("a", acc2[1][2], wd[12][2], H[12]);
  SB(); GM();
  WAITL(2);
  MF16("v", acc2[1][3], xf[2], H[12]);
  SB(); GM();
  WAITV(6);
  MF16("a", acc2[1][0], wd[13][0], H[13]);
  SB(); GM();
  MF16("a", acc2[1][1], wd[13][1], H[13]);
  DSR128(xf[5], wd_rd, 1024);
  { const unsigned so_ = sHl + 1 * H_LAYER + 3674112u; BLOAD4(H[4], v_l16, so_, 0); }
  SB(); GM();
  MF16("a", acc2[1][2], wd[13][2], H[13]);
  SB(); GM();
  WAITL(2);
  MF16("v", acc2[1][3], xf[3], H[13]);
  SB(); GM();
  { const unsigned so_ = sHl + 1 * H_LAYER + 3674112u; BLOAD4(H[5], v_l16, so_, 1024); }
  SB(); GM();
  __builtin_amdgcn_s_sleep(1);
  asm volatile("s_nop 7");
  DSW128(red_wr, acc2[1][0], 0);
  SB(); GM();
  { const unsigned so_ = sHl + 1 * H_LAYER + 3674112u; BLOAD4(H[6], v_l16, so_, 2048); }
  SB(); GM();
  DSW128(rw1, acc2[1][1], 0);
  SB(); GM();
  DSW128(rw2, acc2[1][2], 0);
  SB(); GM();
  DSW128(rw3, acc2[1][3], 0);
  SB(); GM();
  { const unsigned so_ = sHl + 1 * H_LAYER + 3674112u; BLOAD4(H[7], v_l16, so_, 3072); }
  SB(); GM();
  SB(); GM();
  { const unsigned so_ = sHl + 1 * H_LAYER + 3678208u; BLOAD4(H[8], v_l16, so_, 0); }
  SB(); GM();
  WAITL(0);
  SBAR();
  DSR128(pr[0], red_rd, 0);
  DSR128(pr[1], red_rd, 4096);
  DSR128(pr[2], red_rd, 8192);
  DSR128(pr[3], red_rd, 12288);
  SB(); GM();
  { const unsigned so_ = sHl + 1 * H_LAYER + 3678208u; BLOAD4(H[9], v_l16, so_, 1024); }
  SB(); GM();
  WAITL(0);
  asm volatile("v_add_f32 %0, %4, %5\n\tv_add_f32 %1, %6, %7\n\tv_add_f32 %2, %8, %9\n\tv_add_f32 %3, %10, %11\n\tv_add_f32 %0, %0, %12\n\tv_add_f32 %1, %1, %13\n\tv_add_f32 %2, %2, %14" : "=&v"(fo0), "=&v"(fo1), "=&v"(fo2), "=&v"(fo3) : "v"(pr[0][0]), "v"(pr[1][0]), "v"(pr[0][1]), "v"(pr[1][1]), "v"(pr[0][2]), "v"(pr[1][2]), "v"(pr[0][3]), "v"(pr[1][3]), "v"(pr[2][0]), "v"(pr[2][1]), "v"(pr[2][2]));
  SB(); GM();
  asm volatile("v_add_f32 %0, %0, %4\n\tv_add_f32 %1, %1, %5\n\tv_add_f32 %2, %2, %6\n\tv_add_f32 %3, %3, %7\n\tv_add_f32 %0, %0, %8\n\tv_fma_f32 %1, %1, %9, %10\n\tv_fma_f32 %2, %2, %9, %11" : "+v"(fo3), "+v"(fo0), "+v"(fo1), "+v"(fo2) : "v"(pr[2][3]), "v"(pr[3][0]), "v"(pr[3][1]), "v"(pr[3][2]), "v"(pr[3][3]), "v"(rsq5), "v"(rf0), "v"(rf1));
  asm volatile("v_fma_f32 %0, %0, %1, %2" : "+v"(fo2) : "v"(rsq5), "v"(rf2));
  SB(); GM();
  asm volatile("v_fma_f32 %0, %0, %3, %4\n\tv_cvt_pk_bf16_f32 %1, %5, %6\n\tv_cvt_pk_bf16_f32 %2, %7, %0" : "+v"(fo3), "=&v"(fp0), "=&v"(fp1) : "v"(rsq5), "v"(rf3), "v"(fo0), "v"(fo1), "v"(fo2));
  { xo = v2u{fp0, fp1}; const unsigned so_ = sOut + 458752u; OSTORE2(xo, v_out, so_); }
  SB(); GM();
  WAITV(10);
  MF16Z("a", acc2[0][0], wd[0][0], H[0]);
  asm volatile("v_lshlrev_b32 %0, 16, %1" : "=v"(rf0) : "v"(x3p2_0));
  SB(); GM();
  MF16Z("a", acc2[0][1], wd[0][1], H[0]);
  DSR128(xf[6], wd_rd, 2048);
  asm volatile("v_and_b32 %0, 0xffff0000, %1" : "=v"(rf1) : "v"(x3p2_0));
  SB(); STUB(16, ".Lwret16"); GM();
  MF16Z("a", acc2[0][2], wd[0][2], H[0]);
  asm volatile("v_lshlrev_b32 %0, 16, %2\n\tv_and_b32 %1, 0xffff0000, %2" : "=&v"(rf2), "=&v"(rf3) : "v"(x3p2_1));
  SB(); GM();
  MF16Z("v", acc2[0][3], xf[4], H[0]);
  asm volatile("v_xor_b32 %0, 64, %2\n\tv_xor_b32 %1, 128, %2" : "=&v"(rw1), "=&v"(rw2) : "v"(red_wr));
  SB(); GM();
  WAITV(9);
  MF16("a", acc2[0][0], wd[1][0], H[1]);
  asm volatile("v_xor_b32 %0, 192, %1" : "=v"(rw3) : "v"(red_wr));
  SB(); GM();
  MF16("a", acc2[0][1], wd[1][1], H[1]);
  DSR128(xf[7], wd_rd, 3072);
  SBAR();
  SB(); GM();
  MF16("a", acc2[0][2], wd[1][2], H[1]);
  SB(); GM();
  MF16("v", acc2[0][3], xf[5], H[1]);
  SB(); GM();
  WAITV(8);
  MF16("a", acc2[0][0], wd[2][0], H[2]);
  SB(); GM();
  MF16("a", acc2[0][1], wd[2][1], H[2]);
  DSR128(xf[0], wd_rd, 4096);
  SB(); GM();
  MF16("a", acc2[0][2], wd[2][2], H[2]);
  { const unsigned so_ = sHp + 1605632u; BLOAD2(pollh, v_ph, so_); }
  { const unsigned so_ = sHl + 1 * H_LAYER + 3678208u; BLOAD4(H[10], v_l16, so_, 2048); }
  { const unsigned so_ = sHl + 1 * H_LAYER + 3678208u; BLOAD4(H[11], v_l16, so_, 3072); }
  SB(); GM();
  WAITL(2);
  MF16("v", acc2[0][3], xf[6], H[2]);
  { const unsigned so_ = sHl + 1 * H_LAYER + 3682304u; BLOAD4(H[12], v_l16, so_, 0); }
  SB(); GM();
  WAITV(11);
  MF16("a", acc2[0][0], wd[3][0], H[3]);
  SB(); GM();
  MF16("a", acc2[0][1], wd[3][1], H[3]);
  DSR128(xf[1], wd_rd, 5120);
  SB(); GM();
  MF16("a", acc2[0][2], wd[3][2], H[3]);
  SB(); GM();
  WAITL(2);
  MF16("v", acc2[0][3], xf[7], H[3]);
  { const unsigned so_ = sHl + 1 * H_LAYER + 3682304u; BLOAD4(H[13], v_l16, so_, 1024); }
  SB(); GM();
  WAITV(11);
  MF16("a", acc2[0][0], wd[4][0], H[4]);
  SB(); GM();
  MF16("a", acc2[0][1], wd[4][1], H[4]);
  DSR128(xf[2], wd_rd, 6144);
  SB(); GM();
  MF16("a", acc2[0][2], wd[4][2], H[4]);
  SB(); GM();
  WAITL(2);
  MF16("v", acc2[0][3], xf[0], H[4]);
  SB(); GM();
  WAITV(10);
  MF16("a", acc2[0][0], wd[5][0], H[5]);
  SB(); GM();
  MF16("a", acc2[0][1], wd[5][1], H[5]);
  DSR128(xf[3], wd_rd, 7168);
  SB(); GM();
  MF16("a", acc2[0][2], wd[5][2], H[5]);
  SB(); GM();
  WAITL(2);
  MF16("v", acc2[0][3], xf[1], H[5]);
  SB(); GM();
  WAITV(9);
  MF16("a", acc2[0][0], wd[6][0], H[6]);
  SB(); GM();
  MF16("a", acc2[0][1], wd[6][1], H[6]);
  DSR128(xf[4], wd_rd, 8192);
  SB(); GM();
  MF16("a", acc2[0][2], wd[6][2], H[6]);
  SB(); GM();
  WAITL(2);
  MF16("v", acc2[0][3], xf[2], H[6]);
  SB(); GM();
  WAITV(8);
  MF16("a", acc2[0][0], wd[7][0], H[7]);
  SB(); GM();
  MF16("a", acc2[0][1], wd[7][1], H[7]);
  DSR128(xf[5], wd_rd, 9216);
  SB(); GM();
  MF16("a", acc2[0][2], wd[7][2], H[7]);
  SB(); GM();
  WAITL(2);
  MF16("v", acc2[0][3], xf[3], H[7]);
  SB(); GM();
  WAITV(7);
  MF16("a", acc2[0][0], wd[8][0], H[8]);
  SB(); GM();
  MF16("a", acc2[0][1], wd[8][1], H[8]);
  DSR128(xf[6], wd_rd, 10240);
  SB(); GM();
  MF16("a", acc2[0][2], wd[8][2], H[8]);
  SB(); GM();
  WAITL(2);
  MF16("v", acc2[0][3], xf[4], H[8]);
  SB(); GM();
  WAITV(6);
  MF16("a", acc2[0][0], wd[9][0], H[9]);
  WAITV(4);
  { const unsigned so_ = sHp + 1605632u; CHECKF(pollh, v_ph, so_); }
  { const unsigned so_ = sHl + 1 * H_LAYER + 5505024u; BLOAD4(H[0], v_l16, so_, 0); }
  SB(); GM();
  MF16("a", acc2[0][1], wd[9][1], H[9]);
  DSR128(xf[7], wd_rd, 11264);
  { const unsigned so_ = sHl + 1 * H_LAYER + 5505024u; BLOAD4(H[1], v_l16, so_, 1024); }
  { const unsigned so_ = sHl + 1 * H_LAYER + 5505024u; BLOAD4(H[2], v_l16, so_, 2048); }
  SB(); GM();
  MF16("a", acc2[0][2], wd[9][2], H[9]);
  { const unsigned so_ = (b | w) == 0 ? OFF_G : 0xF0000000u; BSTORE2(tagv, v_one, so_); }
  SB(); GM();
  WAITL(2);
  MF16("v", acc2[0][3], xf[5], H[9]);
  SB(); GM();
  { const unsigned so_ = sHl + 1 * H_LAYER + 5505024u; BLOAD4(H[3], v_l16, so_, 3072); }
  SB(); GM();
  { const unsigned so_ = sHl + 1 * H_LAYER + 5509120u; BLOAD4(H[4], v_l16, so_, 0); }
  SB(); GM();
  { const unsigned so_ = sHl + 1 * H_LAYER + 5509120u; BLOAD4(H[5], v_l16, so_, 1024); }
  SB(); GM();
  { const unsigned so_ = sHl + 1 * H_LAYER + 5509120u; BLOAD4(H[6], v_l16, so_, 2048); }
  SB(); GM();
  { const unsigned so_ = sHl + 1 * H_LAYER + 5509120u; BLOAD4(H[7], v_l16, so_, 3072); }
  SB(); GM();
  WAITV(12);
  MF16("a", acc2[0][0], wd[10][0], H[10]);
  { const unsigned so_ = sHl + 1 * H_LAYER + 5513216u; BLOAD4(H[8], v_l16, so_, 0); }
  SB(); GM();
  MF16("a", acc2[0][1], wd[10][1], H[10]);
  DSR128(xf[0], wd_rd, 12288);
  SB(); GM();
  MF16("a", acc2[0][2], wd[10][2], H[10]);
  SB(); GM();
  WAITL(2);
  MF16("v", acc2[0][3], xf[6], H[10]);
  SB(); GM();
  WAITV(12);
  MF16("a", acc2[0][0], wd[11][0], H[11]);
  { const unsigned so_ = sHl + 1 * H_LAYER + 5513216u; BLOAD4(H[9], v_l16, so_, 1024); }
  SB(); GM();
  MF16("a", acc2[0][1], wd[11][1], H[11]);
  DSR128(xf[1], wd_rd, 13312);
  SB(); GM();
  MF16("a", acc2[0][2], wd[11][2], H[11]);
  SB(); GM();
  WAITL(2);
  MF16("v", acc2[0][3], xf[7], H[11]);
  SB(); GM();
  WAITV(12);
  MF16("a", acc2[0][0], wd[12][0], H[12]);
  { const unsigned so_ = sHl + 1 * H_LAYER + 5513216u; BLOAD4(H[10], v_l16, so_, 2048); }
  SB(); GM();
  MF16("a", acc2[0][1], wd[12][1], H[12]);
  DSR128(xf[2], wd_rd, 0);
  SB(); GM();
  MF16("a", acc2[0][2], wd[12][2], H[12]);
  SB(); GM();
  WAITL(2);
  MF16("v", acc2[0][3], xf[0], H[12]);
  SB(); GM();
  WAITV(12);
  MF16("a", acc2[0][0], wd[13][0], H[13]);
  { const unsigned so_ = sHl + 1 * H_LAYER + 5513216u; BLOAD4(H[11], v_l16, so_, 3072); }
  SB(); GM();
  MF16("a", acc2[0][1], wd[13][1], H[13]);
  DSR128(xf[3], wd_rd, 1024);
  SB(); GM();
  MF16("a", acc2[0][2], wd[13][2], H[13]);
  SB(); GM();
  WAITL(2);
  MF16("v", acc2[0][3], xf[1], H[13]);
  SB(); GM();
  { const unsigned so_ = sHl + 1 * H_LAYER + 5517312u; BLOAD4(H[12], v_l16, so_, 0); }
  SB(); GM();
  __builtin_amdgcn_s_sleep(1);
  asm volatile("s_nop 7");
  DSW128(red_wr, acc2[0][0], 0);
  SB(); GM();
  { const unsigned so_ = sHl + 1 * H_LAYER + 5517312u; BLOAD4(H[13], v_l16, so_, 1024); }
  SB(); GM();
  DSW128(rw1, acc2[0][1], 0);
  SB(); GM();
  DSW128(rw2, acc2[0][2], 0);
  SB(); GM();
  DSW128(rw3, acc2[0][3], 0);
  SB(); GM();
  WAITV(14);
  MF16Z("a", acc2[1][0], wd[0][0], H[0]);
  SB(); GM();
  MF16Z("a", acc2[1][1], wd[0][1], H[0]);
  DSR128(xf[4], wd_rd, 2048);
  SB(); GM();
  MF16Z("a", acc2[1][2], wd[0][2], H[0]);
  SB(); GM();
  WAITL(6);
  MF16Z("v", acc2[1][3], xf[2], H[0]);
  SB(); GM();
  WAITV(13);
  MF16("a", acc2[1][0], wd[1][0], H[1]);
  SB(); GM();
  MF16("a", acc2[1][1], wd[1][1], H[1]);
  DSR128(xf[5], wd_rd, 3072);
  SB(); GM();
  MF16("a", acc2[1][2], wd[1][2], H[1]);
  WAITL(2);
  SBAR();
  SB(); GM();
  MF16("v", acc2[1][3], xf[3], H[1]);
  DSR128(pr[0], red_rd, 0);
  DSR128(pr[1], red_rd, 4096);
  SB(); GM();
  WAITV(12);
  MF16("a", acc2[1][0], wd[2][0], H[2]);
  DSR128(pr[2], red_rd, 8192);
  SB(); GM();
  MF16("a", acc2[1][1], wd[2][1], H[2]);
  DSR128(xf[6], wd_rd, 4096);
  DSR128(pr[3], red_rd, 12288);
  SB(); GM();
  MF16("a", acc2[1][2], wd[2][2], H[2]);
  SB(); GM();
  WAITL(6);
  MF16("v", acc2[1][3], xf[4], H[2]);
  SB(); GM();
  WAITV(10);
  MF16("a", acc2[1][0], wd[3][0], H[3]);
  SB(); GM();
  MF16("a", acc2[1][1], wd[3][1], H[3]);
  DSR128(xf[7], wd_rd, 5120);
  SB(); GM();
  MF16("a", acc2[1][2], wd[3][2], H[3]);
  WAITL(1);
  asm volatile("v_add_f32 %0, %1, %2" : "=v"(fo0) : "v"(pr[0][0]), "v"(pr[1][0]));
  SB(); GM();
  MF16("v", acc2[1][3], xf[5], H[3]);
  asm volatile("v_add_f32 %0, %2, %3\n\tv_add_f32 %1, %4, %5" : "=&v"(fo1), "=&v"(fo2) : "v"(pr[0][1]), "v"(pr[1][1]), "v"(pr[0][2]), "v"(pr[1][2]));
  SB(); GM();
  WAITV(9);
  MF16("a", acc2[1][0], wd[4][0], H[4]);
  asm volatile("v_add_f32 %0, %1, %2" : "=v"(fo3) : "v"(pr[0][3]), "v"(pr[1][3]));
  SB(); GM();
  MF16("a", acc2[1][1], wd[4][1], H[4]);
  DSR128(xf[0], wd_rd, 6144);
  asm volatile("v_add_f32 %0, %0, %1" : "+v"(fo0) : "v"(pr[2][0]));
  SB(); GM();
  MF16("a", acc2[1][2], wd[4][2], H[4]);
  asm volatile("v_add_f32 %0, %0, %2\n\tv_add_f32 %1, %1, %3" : "+v"(fo1), "+v"(fo2) : "v"(pr[2][1]), "v"(pr[2][2]));
  SB(); GM();
  MF16("v", acc2[1][3], xf[6], H[4]);
  asm volatile("v_add_f32 %0, %0, %2\n\tv_add_f32 %1, %1, %3" : "+v"(fo3), "+v"(fo0) : "v"(pr[2][3]), "v"(pr[3][0]));
  SB(); GM();
  WAITV(8);
  MF16("a", acc2[1][0], wd[5][0], H[5]);
  asm volatile("v_add_f32 %0, %0, %1" : "+v"(fo1) : "v"(pr[3][1]));
  SB(); GM();
  MF16("a", acc2[1][1], wd[5][1], H[5]);
  DSR128(xf[1], wd_rd, 7168);
  asm volatile("v_add_f32 %0, %0, %1" : "+v"(fo2) : "v"(pr[3][2]));
  SB(); GM();
  MF16("a", acc2[1][2], wd[5][2], H[5]);
  asm volatile("v_add_f32 %0, %0, %2\n\tv_fma_f32 %1, %1, %3, %4" : "+v"(fo3), "+v"(fo0) : "v"(pr[3][3]), "v"(rsq6), "v"(rf0));
  SB(); GM();
  WAITL(2);
  MF16("v", acc2[1][3], xf[7], H[5]);
  asm volatile("v_fma_f32 %0, %0, %1, %2" : "+v"(fo1) : "v"(rsq6), "v"(rf1));
  SB(); GM();
  WAITV(7);
  MF16("a", acc2[1][0], wd[6][0], H[6]);
  asm volatile("v_fma_f32 %0, %0, %1, %2" : "+v"(fo2) : "v"(rsq6), "v"(rf2));
  SB(); GM();
  MF16("a", acc2[1][1], wd[6][1], H[6]);
  DSR128(xf[2], wd_rd, 8192);
  asm volatile("v_fma_f32 %0, %0, %1, %2" : "+v"(fo3) : "v"(rsq6), "v"(rf3));
  SB(); GM();
  MF16("a", acc2[1][2], wd[6][2], H[6]);
  asm volatile("v_cvt_pk_bf16_f32 %0, %2, %3\n\tv_cvt_pk_bf16_f32 %1, %4, %5" : "=&v"(fp0), "=&v"(fp1) : "v"(fo0), "v"(fo1), "v"(fo2), "v"(fo3));
  SB(); GM();
  WAITL(2);
  MF16("v", acc2[1][3], xf[0], H[6]);
  { xo = v2u{fp0, fp1}; const unsigned so_ = sOut + 917504u; OSTORE2(xo, v_out, so_); }
  SB(); GM();
  WAITV(7);
  MF16("a", acc2[1][0], wd[7][0], H[7]);
  asm volatile("v_lshlrev_b32 %0, 16, %1" : "=v"(rf0) : "v"(x3p3_0));
  SB(); GM();
  MF16("a", acc2[1][1], wd[7][1], H[7]);
  DSR128(xf[3], wd_rd, 9216);
  asm volatile("v_and_b32 %0, 0xffff0000, %1" : "=v"(rf1) : "v"(x3p3_0));
  SB(); GM();
  MF16("a", acc2[1][2], wd[7][2], H[7]);
  asm volatile("v_lshlrev_b32 %0, 16, %2\n\tv_and_b32 %1, 0xffff0000, %2" : "=&v"(rf2), "=&v"(rf3) : "v"(x3p3_1));
  SB(); GM();
  WAITL(2);
  MF16("v", acc2[1][3], xf[1], H[7]);
  asm volatile("v_xor_b32 %0, 64, %1" : "=v"(rw1) : "v"(red_wr));
  SB(); GM();
  WAITV(6);
  MF16("a", acc2[1][0], wd[8][0], H[8]);
  asm volatile("v_xor_b32 %0, 128, %1" : "=v"(rw2) : "v"(red_wr));
  SB(); GM();
  MF16("a", acc2[1][1], wd[8][1], H[8]);
  DSR128(xf[4], wd_rd, 10240);
  asm volatile("v_xor_b32 %0, 192, %1" : "=v"(rw3) : "v"(red_wr));
  SB(); GM();
  MF16("a", acc2[1][2], wd[8][2], H[8]);
  SBAR();
  SB(); GM();
  WAITL(2);
  MF16("v", acc2[1][3], xf[2], H[8]);
  SB(); GM();
  WAITV(5);
  MF16("a", acc2[1][0], wd[9][0], H[9]);
  SB(); GM();
  MF16("a", acc2[1][1], wd[9][1], H[9]);
  DSR128(xf[5], wd_rd, 11264);
  SB(); GM();
  MF16("a", acc2[1][2], wd[9][2], H[9]);
  SB(); GM();
  WAITL(2);
  MF16("v", acc2[1][3], xf[3], H[9]);
  SB(); GM();
  WAITV(4);
  MF16("a", acc2[1][0], wd[10][0], H[10]);
  SB(); GM();
  MF16("a", acc2[1][1], wd[10][1], H[10]);
  DSR128(xf[6], wd_rd, 12288);
  SB(); GM();
  MF16("a", acc2[1][2], wd[10][2], H[10]);
  SB(); GM();
  WAITL(2);
  MF16("v", acc2[1][3], xf[4], H[10]);
  SB(); GM();
  WAITV(3);
  MF16("a", acc2[1][0], wd[11][0], H[11]);
  SB(); GM();
  MF16("a", acc2[1][1], wd[11][1], H[11]);
  DSR128(xf[7], wd_rd, 13312);
  SB(); GM();
  MF16("a", acc2[1][2], wd[11][2], H[11]);
  SB(); GM();
  WAITL(2);
  MF16("v", acc2[1][3], xf[5], H[11]);
  SB(); GM();
  WAITV(2);
  MF16("a", acc2[1][0], wd[12][0], H[12]);
  SB(); GM();
  MF16("a", acc2[1][1], wd[12][1], H[12]);
  SB(); GM();
  MF16("a", acc2[1][2], wd[12][2], H[12]);
  SB(); GM();
  WAITL(1);
  MF16("v", acc2[1][3], xf[6], H[12]);
  SB(); GM();
  WAITV(1);
  MF16("a", acc2[1][0], wd[13][0], H[13]);
  SB(); GM();
  MF16("a", acc2[1][1], wd[13][1], H[13]);
  SB(); GM();
  MF16("a", acc2[1][2], wd[13][2], H[13]);
  SB(); GM();
  WAITL(0);
  MF16("v", acc2[1][3], xf[7], H[13]);
  SB(); GM();
  NOPS();
  __builtin_amdgcn_s_sleep(1);
  DSW128(red_wr, acc2[1][0], 0);
  SB(); GM();
  DSW128(rw1, acc2[1][1], 0);
  SB(); GM();
  DSW128(rw2, acc2[1][2], 0);
  SB(); GM();
  DSW128(rw3, acc2[1][3], 0);
  SB(); GM();
  SB(); GM();
  WAITL(0);
  SBAR();
  DSR128(pr[0], red_rd, 0);
  DSR128(pr[1], red_rd, 4096);
  DSR128(pr[2], red_rd, 8192);
  DSR128(pr[3], red_rd, 12288);
  SB(); GM();
  WAITL(0);
  asm volatile("v_add_f32 %0, %4, %5\n\tv_add_f32 %1, %6, %7\n\tv_add_f32 %2, %8, %9\n\tv_add_f32 %3, %10, %11\n\tv_add_f32 %0, %0, %12\n\tv_add_f32 %1, %1, %13\n\tv_add_f32 %2, %2, %14" : "=&v"(fo0), "=&v"(fo1), "=&v"(fo2), "=&v"(fo3) : "v"(pr[0][0]), "v"(pr[1][0]), "v"(pr[0][1]), "v"(pr[1][1]), "v"(pr[0][2]), "v"(pr[1][2]), "v"(pr[0][3]), "v"(pr[1][3]), "v"(pr[2][0]), "v"(pr[2][1]), "v"(pr[2][2]));
  SB(); GM();
  asm volatile("v_add_f32 %0, %0, %4\n\tv_add_f32 %1, %1, %5\n\tv_add_f32 %2, %2, %6\n\tv_add_f32 %3, %3, %7\n\tv_add_f32 %0, %0, %8\n\tv_fma_f32 %1, %1, %9, %10\n\tv_fma_f32 %2, %2, %9, %11" : "+v"(fo3), "+v"(fo0), "+v"(fo1), "+v"(fo2) : "v"(pr[2][3]), "v"(pr[3][0]), "v"(pr[3][1]), "v"(pr[3][2]), "v"(pr[3][3]), "v"(rsq7), "v"(rf0), "v"(rf1));
  asm volatile("v_fma_f32 %0, %0, %1, %2" : "+v"(fo2) : "v"(rsq7), "v"(rf2));
  SB(); GM();
  asm volatile("v_fma_f32 %0, %0, %3, %4\n\tv_cvt_pk_bf16_f32 %1, %5, %6\n\tv_cvt_pk_bf16_f32 %2, %7, %0" : "+v"(fo3), "=&v"(fp0), "=&v"(fp1) : "v"(rsq7), "v"(rf3), "v"(fo0), "v"(fo1), "v"(fo2));
  { xo = v2u{fp0, fp1}; const unsigned so_ = sOut + 1376256u; OSTORE2(xo, v_out, so_); }
  SB(); GM();
  asm volatile(".subsection 2\n.Lcode_end:\n\ts_endpgm\n\t.subsection 0");
}

extern "C" void kernel_launch(void* const* d_in, const int* in_sizes, int n_in, void* d_out, int out_size, void* d_ws, size_t ws_size,
                              hipStream_t stream) {
  P p;
  p.q = (const u16*)d_in[0]; p.k = (const u16*)d_in[1]; p.v = (const u16*)d_in[2]; p.sk = (const u16*)d_in[3];
  p.hk = (const u16*)d_in[4]; p.sD = (const u16*)d_in[5]; p.Wg = (const u16*)d_in[7]; p.Wu = (const u16*)d_in[8]; p.Wd = (const u16*)d_in[9];
  p.out = (u16*)d_out; p.ws = (unsigned char*)d_ws;
  (void)hipFuncSetAttribute((const void*)k_main, hipFuncAttributeMaxDynamicSharedMemorySize, L_TOTAL);
  k_main<<<256, 256, L_TOTAL, stream>>>(p);
}
```
